```python
import torch

def _softmax(x, axis=-1):
    return torch.softmax(x, dim=axis)
def _gelu(x):
    return torch.nn.functional.gelu(x, approximate="tanh")
def _silu(x):
    return torch.nn.functional.silu(x)
def _sigmoid(x):
    return torch.sigmoid(x)
def _layernorm(x, axis=-1, eps=1e-5):
    m = x.mean(dim=axis, keepdim=True)
    v = x.var(dim=axis, keepdim=True, unbiased=False)
    return (x - m) / torch.sqrt(v + eps)
def _scatter_add(tgt, idx, src, axis=0):
    return tgt.clone().index_add_(axis, idx, src)
def _segment_sum(src, seg, n):
    out = torch.zeros((n,) + src.shape[1:], dtype=src.dtype)
    return out.index_add_(0, seg, src)
def _pad(x, pw):
    flat = tuple(v for pair in reversed(pw) for v in pair)
    return torch.nn.functional.pad(x, flat)

kernel_name = 'concat_heads__compressed_kv_cla__ssm_conv_s4__prompt'

def setup_inputs():

    _w = (lambda a: (lambda t: t.to(torch.bfloat16) if t.is_floating_point() else t)(torch.as_tensor(a)))
    def _A_setup():
        B, H, Sq, Sk, D, M = 1, 32, 2048, 2048, 128, 512
        Dc = 4 * D
        return {
            "x": _w((lambda *s: torch.randn(*s, dtype=torch.bfloat16) * 3.2)(B, Sq, M)),
            "c_kv": _w((lambda *s: torch.randn(*s, dtype=torch.bfloat16) * 0.1)(B, Sk, Dc)),
            "w_q": _w((lambda *s: torch.randn(*s, dtype=torch.bfloat16) * 6.4)(M, H, D) * (M ** -0.5)),
            "w_uk": _w((lambda *s: torch.randn(*s, dtype=torch.bfloat16) * 6.4)(Dc, H, D) * (Dc ** -0.5)),
            "w_uv": _w((lambda *s: torch.randn(*s, dtype=torch.bfloat16) * 0.1)(Dc, H, D) * (Dc ** -0.5)),
            "w_o": _w((lambda *s: torch.randn(*s, dtype=torch.bfloat16) * 0.1)(H, D, M) * ((H * D) ** -0.5)),
        }
    _A_in = _A_setup()

    def _B_setup():
        B, L, D, N = 1, 2048, 512, 128
        A_re = -(lambda _x: torch.exp(torch.as_tensor(_x)))((lambda *s: torch.randn(*s, dtype=torch.bfloat16) * 3.2)(D, N))
        A_im = torch.pi * (lambda *s: torch.randn(*s, dtype=torch.bfloat16) * 0.05)(D, N)
        B_re = (lambda *s: torch.randn(*s, dtype=torch.bfloat16) * 0.1)(D, N)
        B_im = (lambda *s: torch.randn(*s, dtype=torch.bfloat16) * 0.1)(D, N)
        C_re = (lambda *s: torch.randn(*s, dtype=torch.bfloat16) * 0.1)(D, N)
        C_im = (lambda *s: torch.randn(*s, dtype=torch.bfloat16) * 0.1)(D, N)
        log_dt = (lambda *s: torch.randn(*s, dtype=torch.bfloat16) * 0.05)(D) * 0.5 - 4.0
        Dskip = (lambda *s: torch.randn(*s, dtype=torch.bfloat16) * 0.05)(D)
        return {
            "u": _w((lambda *s: torch.randn(*s, dtype=torch.bfloat16) * 0.05)(B, L, D)),
            "A_re": _w(A_re),
            "A_im": _w(A_im),
            "B_re": _w(B_re),
            "B_im": _w(B_im),
            "C_re": _w(C_re),
            "C_im": _w(C_im),
            "log_dt": _w(log_dt),
            "Dskip": _w(Dskip),
        }
    _B_in = _B_setup()

    return {
        "A_x": _A_in["x"],
        "A_c_kv": _A_in["c_kv"],
        "A_w_q": _A_in["w_q"],
        "A_w_uk": _A_in["w_uk"],
        "A_w_uv": _A_in["w_uv"],
        "A_w_o": _A_in["w_o"],
        "B_u": _B_in["u"],
        "B_A_re": _B_in["A_re"],
        "B_A_im": _B_in["A_im"],
        "B_B_re": _B_in["B_re"],
        "B_B_im": _B_in["B_im"],
        "B_C_re": _B_in["C_re"],
        "B_C_im": _B_in["C_im"],
        "B_log_dt": _B_in["log_dt"],
        "B_Dskip": _B_in["Dskip"],
    }

def reference(A_x, A_c_kv, A_w_q, A_w_uk, A_w_uv, A_w_o, B_u, B_A_re, B_A_im, B_B_re, B_B_im, B_C_re, B_C_im, B_log_dt, B_Dskip):
    def _A_ref(x, c_kv, w_q, w_uk, w_uv, w_o):
        B, Sq, M = x.shape[0], x.shape[1], x.shape[2]
        Sk = c_kv.shape[1]
        H, D = w_q.shape[1], w_q.shape[2]
        q = torch.einsum("bqm,mhd->bhqd", x, w_q)
        k = torch.einsum("bkc,chd->bhkd", c_kv, w_uk)
        v = torch.einsum("bkc,chd->bhkd", c_kv, w_uv)
        scores = torch.einsum("bhqd,bhkd->bhqk", q, k) * (D ** -0.5)
        off = Sk - Sq
        qi = torch.arange(Sq)[:, None]
        kj = torch.arange(Sk)[None, :]
        cmask = kj <= (qi + off)
        scores = torch.where(cmask[None, None], scores, -1e9)
        p = _softmax(scores, axis=-1)
        ctx = torch.einsum("bhqk,bhkd->bhqd", p, v)
        return torch.einsum("bhqd,hdm->bqm", ctx, w_o)

    def _B_ref(u, A_re, A_im, B_re, B_im, C_re, C_im, log_dt, Dskip):
        Bsz, L, D = u.shape
        dt = torch.exp(log_dt)[:, None]
        A = A_re + 1j * A_im
        Bc = B_re + 1j * B_im
        Cc = C_re + 1j * C_im
        logdA = dt * A
        dB = dt * Bc
        l = (lambda x, dt: x.to(dt))(torch.arange(L), A_re.dtype)
        P = torch.exp(logdA[:, :, None] * l[None, None, :])
        K = torch.einsum("dn,dnl,dn->dl", Cc, P, dB).real
        L2 = 2 * L
        Kp = _pad(K, ((0, 0), (0, L)))
        up = (lambda x, *d: x.permute(*d))(u, 0, 2, 1)
        up = _pad(up, ((0, 0), (0, 0), (0, L)))
        Kf = (lambda x, axes=(-2,-1): torch.fft.rfft2(x.float(), dim=axes))(Kp[:, None, :], axes=(-2, -1))
        Uf = (lambda x, axes=(-2,-1): torch.fft.rfft2(x.float(), dim=axes))(up[:, :, None, :], axes=(-2, -1))
        Yf = Uf * Kf[None, :, :, :]
        y = (lambda x, s, axes=(-2,-1): torch.fft.irfft2(x, s=s, dim=axes).to(torch.bfloat16))(Yf, (1, L2), axes=(-2, -1))[:, :, 0, :L]
        y = (lambda x, *d: x.permute(*d))(y, 0, 2, 1) + u * Dskip[None, None, :]
        return y

    a = _A_ref(A_x, A_c_kv, A_w_q, A_w_uk, A_w_uv, A_w_o)
    b = _B_ref(B_u, B_A_re, B_A_im, B_B_re, B_B_im, B_C_re, B_C_im, B_log_dt, B_Dskip)
    return torch.concatenate([a, b], axis=-1)
```

```cpp
#include <hip/hip_runtime.h>
#include <hip/hip_bf16.h>
#include <stdint.h>

typedef __bf16 bf16x8 __attribute__((ext_vector_type(8)));
typedef __bf16 bf16x4 __attribute__((ext_vector_type(4)));
typedef __bf16 bf16x2 __attribute__((ext_vector_type(2)));
typedef short s16x4 __attribute__((ext_vector_type(4)));
typedef float f32x16 __attribute__((ext_vector_type(16)));
typedef float f32x4 __attribute__((ext_vector_type(4)));
typedef float f32x2 __attribute__((ext_vector_type(2)));
typedef unsigned u32x4 __attribute__((ext_vector_type(4)));
typedef unsigned u32x2 __attribute__((ext_vector_type(2)));
#define LAS __attribute__((address_space(3)))
#define DI __device__ __forceinline__
#define MFMA32(a, b, c) __builtin_amdgcn_mfma_f32_32x32x16_bf16((a), (b), (c), 0, 0, 0)

constexpr int SEQ = 2048, NH = 32, HD = 128, DM = 512;
constexpr float QSCALE = 0.08838834764831845f * 1.4426950408889634f;
constexpr float THR = 8.0f;

constexpr size_t WS_Q = 0;
constexpr size_t WS_KV = 16u << 20;
constexpr size_t WS_CTX = 48u << 20;
constexpr size_t WS_WO = 64u << 20;

DI unsigned cvtpk(float lo, float hi) { f32x2 v = {lo, hi}; bf16x2 b = __builtin_convertvector(v, bf16x2); return __builtin_bit_cast(unsigned, b); }
DI float bf2f(unsigned short b) { return __uint_as_float(((unsigned)b) << 16); }
DI float bfround(float x) { __bf16 b = (__bf16)x; return (float)b; }
DI unsigned rfl(unsigned x) { return __builtin_amdgcn_readfirstlane(x); }
DI bf16x8 pack8(const f32x16& x, int s, float sc) {
    u32x4 w = {cvtpk(x[8 * s + 0] * sc, x[8 * s + 1] * sc), cvtpk(x[8 * s + 2] * sc, x[8 * s + 3] * sc),
               cvtpk(x[8 * s + 4] * sc, x[8 * s + 5] * sc), cvtpk(x[8 * s + 6] * sc, x[8 * s + 7] * sc)};
    return __builtin_bit_cast(bf16x8, w);
}
DI bf16x8 pack8n(const f32x16& x, int s) {
    u32x4 w = {cvtpk(x[8 * s + 0], x[8 * s + 1]), cvtpk(x[8 * s + 2], x[8 * s + 3]), cvtpk(x[8 * s + 4], x[8 * s + 5]), cvtpk(x[8 * s + 6], x[8 * s + 7])};
    return __builtin_bit_cast(bf16x8, w);
}
DI float swap_max(float x) { auto r = __builtin_amdgcn_permlane32_swap(__float_as_uint(x), __float_as_uint(x), false, false); return fmaxf(__uint_as_float(r[0]), __uint_as_float(r[1])); }
DI float swap_sum(float x) { auto r = __builtin_amdgcn_permlane32_swap(__float_as_uint(x), __float_as_uint(x), false, false); return __uint_as_float(r[0]) + __uint_as_float(r[1]); }
DI bf16x8 lds_rd128(unsigned addr) { return *(const LAS bf16x8*)(uintptr_t)addr; }
DI s16x4 lds_tr(unsigned addr) { return __builtin_amdgcn_ds_read_tr16_b64_v4i16((LAS s16x4*)(uintptr_t)addr); }
DI bf16x8 cat8(s16x4 lo, s16x4 hi) { return __builtin_bit_cast(bf16x8, __builtin_shufflevector(lo, hi, 0, 1, 2, 3, 4, 5, 6, 7)); }

DI void glds1(const void* sbase, unsigned voff, unsigned m0v) {
    unsigned keep;
    asm volatile("s_mov_b32 %0, m0\n\ts_mov_b32 m0, %3\n\ts_nop 0\n\tglobal_load_lds_dwordx4 %1, %2\n\ts_mov_b32 m0, %0"
                 : "=&s"(keep) : "v"(voff), "s"(sbase), "s"(m0v) : "memory");
}
DI void glds4(const void* sbase, unsigned voff, unsigned m0v) {
    unsigned keep;
    asm volatile("s_mov_b32 %0, m0\n\ts_mov_b32 m0, %3\n\ts_nop 0\n\t"
                 "global_load_lds_dwordx4 %1, %2\n\tglobal_load_lds_dwordx4 %1, %2 offset:1024\n\t"
                 "global_load_lds_dwordx4 %1, %2 offset:2048\n\tglobal_load_lds_dwordx4 %1, %2 offset:3072\n\ts_mov_b32 m0, %0"
                 : "=&s"(keep) : "v"(voff), "s"(sbase), "s"(m0v) : "memory");
}
#define SYNC_ALL() asm volatile("s_waitcnt vmcnt(0) lgkmcnt(0)\n\ts_barrier" ::: "memory")
#define SYNC_LDS() asm volatile("s_waitcnt lgkmcnt(0)\n\ts_barrier" ::: "memory")

__global__ __launch_bounds__(512, 2) void proj_kernel(const unsigned short* __restrict__ x, const unsigned short* __restrict__ ckv,
                                                      const unsigned short* __restrict__ wq, const unsigned short* __restrict__ wuk,
                                                      const unsigned short* __restrict__ wuv, const unsigned short* __restrict__ wo,
                                                      char* __restrict__ ws) {
    __shared__ __attribute__((aligned(16))) char lds[131072];
    const int tid = threadIdx.x, lane = tid & 63, w = rfl(tid >> 6), r32 = lane & 31, hh = lane >> 5;
    const int b = blockIdx.x, xcd = b & 7, idx = b >> 3, h = xcd * 4 + (idx & 3), rb = idx >> 2;
    const unsigned lb = (unsigned)(uintptr_t)lds;
    const int r8 = lane >> 3, sl8 = lane & 7;
    const unsigned xoff0 = r8 * 1024 + ((sl8 ^ (r8 >> 1)) << 4);
    const unsigned xoff1 = r8 * 1024 + ((sl8 ^ (4 + (r8 >> 1))) << 4);
    const int r4 = lane >> 4, sl16 = lane & 15;
    const unsigned woff = r4 * 8192 + ((sl16 ^ (r4 << 2)) << 4);
    auto stage = [&](int t) {
        const unsigned buf = lb + (t & 1) * 65536;
        const int kt = t & 7;
        const char* xs = (const char*)(t < 8 ? ckv : x) + (size_t)(rb * 256 + w * 32) * 1024 + kt * 128;
#pragma unroll
        for (int q = 0; q < 4; ++q) glds1(xs + q * 8192, (q & 1) ? xoff1 : xoff0, buf + (w * 4 + q) * 1024);
        const size_t wo_ = (size_t)(kt * 64 + w * 8) * 8192 + h * 256;
        const char* w1 = (const char*)(t < 8 ? wuk : wq) + wo_;
#pragma unroll
        for (int q = 0; q < 2; ++q) glds1(w1 + q * 32768, woff, buf + 32768 + (w * 2 + q) * 1024);
        if (t < 8) {
            const char* w2 = (const char*)wuv + wo_;
#pragma unroll
            for (int q = 0; q < 2; ++q) glds1(w2 + q * 32768, woff, buf + 49152 + (w * 2 + q) * 1024);
        }
    };
    stage(0);
    {
        const int kstep = b;
#pragma unroll
        for (int it = 0; it < 2; ++it) {
            const int nb = w + it * 8;
            const unsigned short* src = wo + (size_t)(kstep * 16 + 4 * hh) * DM + nb * 32 + r32;
            unsigned short e[8];
#pragma unroll
            for (int j = 0; j < 8; ++j) e[j] = src[(8 * (j >> 2) + (j & 3)) * DM];
            u32x4 v = {e[0] | ((unsigned)e[1] << 16), e[2] | ((unsigned)e[3] << 16), e[4] | ((unsigned)e[5] << 16), e[6] | ((unsigned)e[7] << 16)};
            *(u32x4*)(ws + WS_WO + ((size_t)(nb * 256 + kstep) * 64 + lane) * 16) = v;
        }
    }
    const int f = (r32 >> 1) & 7;
    unsigned xa[4];
#pragma unroll
    for (int s = 0; s < 4; ++s) xa[s] = r32 * 128 + (((2 * s + hh) ^ f) << 4);
    const int g16 = (lane >> 4) & 1, q4 = (lane & 15) >> 2, p4 = lane & 3;
    auto wbase = [&](int dq) -> unsigned { return (8 * hh + q4) * 256 + (((dq ^ q4) * 4 + g16 * 2 + (p4 >> 1)) << 4) + (p4 & 1) * 8; };
    {
        const int th = w >> 2, dq = w & 3;
        const unsigned wa = wbase(dq);
        f32x16 acck[4], accv[4];
#pragma unroll
        for (int i = 0; i < 4; ++i) { acck[i] = f32x16{}; accv[i] = f32x16{}; }
        for (int t = 0; t < 8; ++t) {
            SYNC_ALL();
            stage(t + 1);
            const unsigned buf = lb + (t & 1) * 65536;
#pragma unroll
            for (int s = 0; s < 4; ++s) {
                const bf16x8 wk = cat8(lds_tr(buf + 32768 + wa + s * 4096), lds_tr(buf + 32768 + wa + s * 4096 + 1024));
                const bf16x8 wv = cat8(lds_tr(buf + 49152 + wa + s * 4096), lds_tr(buf + 49152 + wa + s * 4096 + 1024));
#pragma unroll
                for (int tb = 0; tb < 4; ++tb) {
                    const bf16x8 xf = lds_rd128(buf + xa[s] + (th * 4 + tb) * 4096);
                    acck[tb] = MFMA32(wk, xf, acck[tb]);
                    accv[tb] = MFMA32(xf, wv, accv[tb]);
                }
            }
        }
#pragma unroll
        for (int tb = 0; tb < 4; ++tb) {
            const int kb32 = rb * 8 + th * 4 + tb;
            char* tile = ws + WS_KV + ((size_t)(h * 32 + (kb32 >> 1)) << 15);
#pragma unroll
            for (int sp = 0; sp < 2; ++sp) {
                *(bf16x8*)(tile + (((kb32 & 1) * 8 + dq * 2 + sp) << 10) + lane * 16) = pack8n(acck[tb], sp);
                *(bf16x8*)(tile + 16384 + ((dq * 4 + (kb32 & 1) * 2 + sp) << 10) + lane * 16) = pack8n(accv[tb], sp);
            }
        }
    }
    {
        const int tq = w >> 1, dh = w & 1;
        const unsigned wa0 = wbase(dh * 2), wa1 = wbase(dh * 2 + 1);
        f32x16 acc[2][2];
#pragma unroll
        for (int i = 0; i < 2; ++i) { acc[i][0] = f32x16{}; acc[i][1] = f32x16{}; }
        for (int t = 8; t < 16; ++t) {
            SYNC_ALL();
            if (t + 1 < 16) stage(t + 1);
            const unsigned buf = lb + (t & 1) * 65536;
#pragma unroll
            for (int s = 0; s < 4; ++s) {
                const bf16x8 w0 = cat8(lds_tr(buf + 32768 + wa0 + s * 4096), lds_tr(buf + 32768 + wa0 + s * 4096 + 1024));
                const bf16x8 w1 = cat8(lds_tr(buf + 32768 + wa1 + s * 4096), lds_tr(buf + 32768 + wa1 + s * 4096 + 1024));
#pragma unroll
                for (int tb = 0; tb < 2; ++tb) {
                    const bf16x8 xf = lds_rd128(buf + xa[s] + (tq * 2 + tb) * 4096);
                    acc[tb][0] = MFMA32(w0, xf, acc[tb][0]);
                    acc[tb][1] = MFMA32(w1, xf, acc[tb][1]);
                }
            }
        }
#pragma unroll
        for (int tb = 0; tb < 2; ++tb) {
            const int qb32 = rb * 8 + tq * 2 + tb;
            char* dst = ws + WS_Q + ((size_t)(h * 64 + qb32) << 13);
#pragma unroll
            for (int db = 0; db < 2; ++db)
#pragma unroll
                for (int sp = 0; sp < 2; ++sp)
                    *(bf16x8*)(dst + (((dh * 2 + db) * 2 + sp) << 10) + lane * 16) = pack8(acc[tb][db], sp, QSCALE);
        }
    }
}

__global__ __launch_bounds__(512, 2) void attn_kernel(char* __restrict__ ws) {
    __shared__ __attribute__((aligned(16))) char lds[163840];
    const int tid = threadIdx.x, lane = tid & 63, w = rfl(tid >> 6), r32 = lane & 31, hh = lane >> 5;
    const int b = blockIdx.x, xcd = b & 7, idx = b >> 3, h = xcd * 4 + (idx & 3), bi = idx >> 2, bj = 15 - bi;
    const int grp = w >> 2, rg = w & 3;
    const int n1 = 2 * (bi + 1);
    const unsigned lb = (unsigned)(uintptr_t)lds;
    const unsigned l16 = lane * 16;
    const char* kvh = ws + WS_KV + ((size_t)h << 20) + w * 4096;
    const char* qh = ws + WS_Q + ((size_t)h << 19);
    auto load_tile = [&](int tau) { glds4(kvh + ((size_t)tau << 15), l16, lb + (tau % 5) * 32768 + w * 4096); };
    auto first_tile = [&](int t) { return t < n1 ? t : 2 * t - n1; };
    auto sync_step = [&](int t) {
        SYNC_ALL();
        if (t < 16) { const int ta = first_tile(t + 1); load_tile(ta); if (t + 1 >= n1) load_tile(ta + 1); }
    };
    load_tile(0);
    int blk = grp ? bj : bi;
    bf16x8 qf[8];
#pragma unroll
    for (int s = 0; s < 8; ++s) qf[s] = *(const bf16x8*)(qh + ((size_t)(blk * 4 + rg) << 13) + s * 1024 + l16);
    f32x16 o[4], negm = f32x16{};
#pragma unroll
    for (int i = 0; i < 4; ++i) o[i] = f32x16{};
    float mref = 0.f, lsum = 0.f;
    bool first = true;
    if (grp) sync_step(0);
    for (int t = 0; t <= 16; ++t) {
        if (!grp) sync_step(t);
        const int tau = first_tile(t) + ((grp && t >= n1) ? 1 : 0);
        const int row0 = blk * 128 + rg * 32;
        const bool active = tau * 64 <= row0 + 31;
        const unsigned slot = lb + (tau % 5) * 32768 + l16;
        f32x16 s0, s1;
        if (active) {
#pragma unroll
            for (int s = 0; s < 8; ++s) {
                const bf16x8 k0 = lds_rd128(slot + s * 1024), k1 = lds_rd128(slot + 8192 + s * 1024);
                s0 = MFMA32(k0, qf[s], s ? s0 : negm);
                s1 = MFMA32(k1, qf[s], s ? s1 : negm);
            }
        }
        const bool sw = (!grp) && (t == n1 - 1);
        if (sw) {
#pragma unroll
            for (int s = 0; s < 8; ++s) qf[s] = *(const bf16x8*)(qh + ((size_t)(bj * 4 + rg) << 13) + s * 1024 + l16);
        }
        if (grp && t < 16) sync_step(t + 1);
        if (active) {
            if (tau * 64 + 63 > row0) {
                const int dq = row0 + r32 - tau * 64 - 4 * hh;
#pragma unroll
                for (int r = 0; r < 16; ++r) {
                    const int c = (r & 3) + 8 * (r >> 2);
                    if (c > dq) s0[r] = -__builtin_inff();
                    if (c + 32 > dq) s1[r] = -__builtin_inff();
                }
            }
            float mx = fmaxf(s0[0], s1[0]);
#pragma unroll
            for (int r = 1; r < 16; ++r) mx = fmaxf(mx, fmaxf(s0[r], s1[r]));
            mx = swap_max(mx);
            if (first || __any(mx > THR)) {
                const float dl = first ? mx : fmaxf(mx, 0.f);
                mref += dl;
#pragma unroll
                for (int r = 0; r < 16; ++r) { s0[r] -= dl; s1[r] -= dl; negm[r] = -mref; }
                if (!first) {
                    const float fs = __builtin_amdgcn_exp2f(-dl);
                    lsum *= fs;
#pragma unroll
                    for (int d = 0; d < 4; ++d)
#pragma unroll
                        for (int r = 0; r < 16; ++r) o[d][r] *= fs;
                }
                first = false;
            }
            float ps0 = 0.f, ps1 = 0.f;
#pragma unroll
            for (int r = 0; r < 16; ++r) {
                s0[r] = __builtin_amdgcn_exp2f(s0[r]); ps0 += s0[r];
                s1[r] = __builtin_amdgcn_exp2f(s1[r]); ps1 += s1[r];
            }
            lsum += ps0 + ps1;
            bf16x8 p[4] = {pack8n(s0, 0), pack8n(s0, 1), pack8n(s1, 0), pack8n(s1, 1)};
#pragma unroll
            for (int ks = 0; ks < 4; ++ks)
#pragma unroll
                for (int db = 0; db < 4; ++db) {
                    const bf16x8 vf = lds_rd128(slot + 16384 + (db * 4 + ks) * 1024);
                    o[db] = MFMA32(vf, p[ks], o[db]);
                }
        }
        if (sw) {
            const float inv = 1.0f / swap_sum(lsum);
            char* dst = ws + WS_CTX + ((size_t)((bi * 4 + rg) * 256 + h * 8) << 10) + l16;
#pragma unroll
            for (int db = 0; db < 4; ++db)
#pragma unroll
                for (int sp = 0; sp < 2; ++sp) *(bf16x8*)(dst + (db * 2 + sp) * 1024) = pack8(o[db], sp, inv);
#pragma unroll
            for (int i = 0; i < 4; ++i) o[i] = f32x16{};
            negm = f32x16{}; mref = 0.f; lsum = 0.f; first = true; blk = bj;
        }
    }
    lsum = swap_sum(lsum);
    SYNC_ALL();
    {
        char* mine = lds + w * 9216;
        const int give = grp ? 0 : 2;
#pragma unroll
        for (int d = 0; d < 2; ++d)
#pragma unroll
            for (int g = 0; g < 4; ++g) {
                const f32x16& src = grp ? o[d] : o[2 + d];
                f32x4 v = {src[4 * g], src[4 * g + 1], src[4 * g + 2], src[4 * g + 3]};
                *(f32x4*)(mine + (d * 4 + g) * 1024 + l16) = v;
            }
        (void)give;
        *(f32x2*)(mine + 8192 + lane * 8) = f32x2{mref, lsum};
    }
    SYNC_LDS();
    {
        const char* oth = lds + (w ^ 4) * 9216;
        const f32x2 ml = *(const f32x2*)(oth + 8192 + lane * 8);
        const float m = fmaxf(mref, ml[0]);
        const float fa = __builtin_amdgcn_exp2f(mref - m), fb = __builtin_amdgcn_exp2f(ml[0] - m);
        const float inv = 1.0f / (lsum * fa + ml[1] * fb);
        const float ca = fa * inv, cb = fb * inv;
        char* dst = ws + WS_CTX + ((size_t)((bj * 4 + rg) * 256 + h * 8 + (grp ? 4 : 0)) << 10) + l16;
#pragma unroll
        for (int d = 0; d < 2; ++d) {
            f32x16 acc = grp ? o[2 + d] : o[d];
#pragma unroll
            for (int g = 0; g < 4; ++g) {
                const f32x4 v = *(const f32x4*)(oth + (d * 4 + g) * 1024 + l16);
#pragma unroll
                for (int e = 0; e < 4; ++e) acc[4 * g + e] = acc[4 * g + e] * ca + v[e] * cb;
            }
#pragma unroll
            for (int sp = 0; sp < 2; ++sp) *(bf16x8*)(dst + (d * 2 + sp) * 1024) = pack8n(acc, sp);
        }
    }
}

__global__ __launch_bounds__(256, 2) void oproj_kernel(const char* __restrict__ ws, unsigned short* __restrict__ out) {
    __shared__ __attribute__((aligned(16))) char lds[65536];
    const int tid = threadIdx.x, lane = tid & 63, w = rfl(tid >> 6), r32 = lane & 31, hh = lane >> 5;
    const int b = blockIdx.x, xcd = b & 7, idx = b >> 3, mb = xcd * 4 + (idx & 3), nb = idx >> 2;
    const char* a0 = ws + WS_CTX + ((size_t)((mb * 2) * 256 + w * 64) << 10) + lane * 16;
    const char* b0 = ws + WS_WO + ((size_t)((nb * 2) * 256 + w * 64) << 10) + lane * 16;
    f32x16 acc[2][2];
#pragma unroll
    for (int i = 0; i < 2; ++i) { acc[i][0] = f32x16{}; acc[i][1] = f32x16{}; }
#pragma unroll 8
    for (int k = 0; k < 64; ++k) {
        const bf16x8 c0 = *(const bf16x8*)(a0 + k * 1024), c1 = *(const bf16x8*)(a0 + 262144 + k * 1024);
        const bf16x8 w0 = *(const bf16x8*)(b0 + k * 1024), w1 = *(const bf16x8*)(b0 + 262144 + k * 1024);
        acc[0][0] = MFMA32(w0, c0, acc[0][0]);
        acc[0][1] = MFMA32(w1, c0, acc[0][1]);
        acc[1][0] = MFMA32(w0, c1, acc[1][0]);
        acc[1][1] = MFMA32(w1, c1, acc[1][1]);
    }
#pragma unroll
    for (int i = 0; i < 4; ++i)
#pragma unroll
        for (int g = 0; g < 4; ++g) {
            const f32x16& s = acc[i >> 1][i & 1];
            *(f32x4*)(lds + w * 16384 + (i * 4 + g) * 1024 + lane * 16) = f32x4{s[4 * g], s[4 * g + 1], s[4 * g + 2], s[4 * g + 3]};
        }
    __syncthreads();
    unsigned pk[8];
#pragma unroll
    for (int g = 0; g < 4; ++g) {
        f32x4 s = *(const f32x4*)(lds + (w * 4 + g) * 1024 + lane * 16);
#pragma unroll
        for (int ww = 1; ww < 4; ++ww) s += *(const f32x4*)(lds + ww * 16384 + (w * 4 + g) * 1024 + lane * 16);
        pk[2 * g] = cvtpk(s[0], s[1]); pk[2 * g + 1] = cvtpk(s[2], s[3]);
    }
    const int q = mb * 64 + (w >> 1) * 32 + r32;
    unsigned short* orow = out + (size_t)q * 1024 + nb * 64 + (w & 1) * 32;
#pragma unroll
    for (int gp = 0; gp < 2; ++gp) {
        auto r0 = __builtin_amdgcn_permlane32_swap(pk[4 * gp + 0], pk[4 * gp + 2], false, false);
        auto r1 = __builtin_amdgcn_permlane32_swap(pk[4 * gp + 1], pk[4 * gp + 3], false, false);
        *(u32x4*)(orow + gp * 16 + hh * 8) = u32x4{r0[0], r1[0], r0[1], r1[1]};
    }
}

struct cf { float r, i; };
DI cf cmul(cf a, cf b) { return cf{a.r * b.r - a.i * b.i, a.r * b.i + a.i * b.r}; }
DI cf cexp_scaled(float a2, float brev, float v) {
    const float e = __builtin_amdgcn_exp2f(a2 * v);
    const float ph = __builtin_amdgcn_fractf(brev * v);
    return cf{e * __builtin_amdgcn_cosf(ph), e * __builtin_amdgcn_sinf(ph)};
}
constexpr int GROW = 1040;
constexpr int SSM_FAC = 64 * GROW;
constexpr int UCOPY = 4160;
constexpr int SSM_U = 8 * UCOPY;
constexpr int SSM_KP = 2 * 4 * 96 * 16;
constexpr int SSM_KD = 2576;
__global__ __launch_bounds__(256, 1) void ssm_kernel(const unsigned short* __restrict__ u, const unsigned short* __restrict__ are,
                                                     const unsigned short* __restrict__ aim, const unsigned short* __restrict__ bre,
                                                     const unsigned short* __restrict__ bim, const unsigned short* __restrict__ cre,
                                                     const unsigned short* __restrict__ cim, const unsigned short* __restrict__ logdt,
                                                     const unsigned short* __restrict__ dskip, unsigned short* __restrict__ out) {
    __shared__ __attribute__((aligned(16))) char lds[2 * SSM_FAC + 2 * SSM_KP + 2 * SSM_KD];
    const int tid = threadIdx.x, lane = tid & 63, w = rfl(tid >> 6), r32 = lane & 31, hh = lane >> 5;
    const int b = blockIdx.x, d0 = (b & 7) * 64 + (b >> 3) * 2;
    const int ch = w >> 1, wi = w & 1;
    char* fac = lds + ch * SSM_FAC;
    char* kp = lds + 2 * SSM_FAC + ch * SSM_KP;
    float* kd = (float*)(lds + 2 * SSM_FAC + 2 * SSM_KP + ch * SSM_KD);
    unsigned up[8];
#pragma unroll
    for (int k = 0; k < 8; ++k) up[k] = *(const unsigned*)(u + (size_t)(tid + 256 * k) * DM + d0);
    {
        const int n = tid & 127, d = d0 + ch;
        const float dt = bfround(expf(bf2f(logdt[d])));
        const int o = d * 128 + n;
        const float a = dt * bf2f(are[o]), bb = dt * bf2f(aim[o]);
        const cf dB = cf{dt * bf2f(bre[o]), dt * bf2f(bim[o])};
        const cf coef = cmul(cf{bf2f(cre[o]), bf2f(cim[o])}, dB);
        const float a2 = a * 1.4426950408889634f, brev = bb * 0.15915494309189535f;
        {
            const cf e8 = cexp_scaled(a2, brev, 8.f);
            cf fcur = cf{1.f, 0.f};
#pragma unroll
            for (int j = 0; j < 16; ++j) {
                *(f32x2*)(fac + (48 + j) * GROW + n * 8) = f32x2{fcur.r, fcur.i};
                fcur = (j == 7) ? cexp_scaled(a2, brev, 64.f) : cmul(fcur, e8);
            }
        }
        cf lo[8];
        lo[0] = cf{1.f, 0.f}; lo[1] = cexp_scaled(a2, brev, 1.f);
#pragma unroll
        for (int c = 2; c < 8; ++c) lo[c] = cmul(lo[c - 1], lo[1]);
        const cf e128 = cexp_scaled(a2, brev, 128.f);
        cf hcur = coef;
        int row = 0;
#pragma unroll
        for (int k = 0; k <= 16; ++k) {
            const int cnt = k < 2 ? 8 : (k < 4 ? 4 : (k < 8 ? 2 : 1));
            const int stp = 8 / cnt;
#pragma unroll
            for (int c = 0; c < cnt; ++c) {
                const cf g = c ? cmul(hcur, lo[c * stp]) : hcur;
                *(f32x2*)(fac + row * GROW + n * 8) = f32x2{g.r, -g.i};
                ++row;
            }
            hcur = ((k & 3) == 3) ? cmul(coef, cexp_scaled(a2, brev, 128.f * (k + 1))) : cmul(hcur, e128);
        }
#pragma unroll
        for (int rr = 41; rr < 48; ++rr) *(f32x2*)(fac + rr * GROW + n * 8) = f32x2{0.f, 0.f};
    }
    __syncthreads();
    {
        const int i16 = lane & 15, g4 = lane >> 4;
        const int t0 = wi ? 2 : 0, nt = wi ? 1 : 2;
        for (int tt = 0; tt < nt; ++tt) {
            const int tile = t0 + tt;
            f32x4 acc = f32x4{};
            f32x4 acc2 = f32x4{};
#pragma unroll 4
            for (int kk = 0; kk < 16; ++kk) {
                const f32x4 ga = *(const f32x4*)(fac + (tile * 16 + i16) * GROW + kk * 64 + g4 * 16);
                const f32x4 fb = *(const f32x4*)(fac + (48 + i16) * GROW + kk * 64 + g4 * 16);
                acc = __builtin_amdgcn_mfma_f32_16x16x4f32(ga[0], fb[0], acc, 0, 0, 0);
                acc2 = __builtin_amdgcn_mfma_f32_16x16x4f32(ga[1], fb[1], acc2, 0, 0, 0);
                acc = __builtin_amdgcn_mfma_f32_16x16x4f32(ga[2], fb[2], acc, 0, 0, 0);
                acc2 = __builtin_amdgcn_mfma_f32_16x16x4f32(ga[3], fb[3], acc2, 0, 0, 0);
            }
#pragma unroll
            for (int e = 0; e < 4; ++e) {
                const int i = tile * 16 + g4 * 4 + e, j = i16;
                int id;
                if (i < 16) id = (i >> 3) * 128 + (i & 7) + 8 * j;
                else if (i < 24) id = 256 + ((i - 16) >> 2) * 64 + ((i - 16) & 3) + 4 * j;
                else if (i < 32) id = 384 + ((i - 24) >> 1) * 32 + ((i - 24) & 1) + 2 * j;
                else id = 512 + (i - 32) * 16 + j;
                if (id <= 640) kd[id] = acc[e] + acc2[e];
            }
        }
    }
    __syncthreads();
    {
        const int t7 = tid & 127;
#pragma unroll
        for (int it = 0; it < 2; ++it) {
            const int c8 = t7 + it * 128;
            unsigned hi[4], lo[4];
#pragma unroll
            for (int e = 0; e < 4; ++e) {
                float kv[2];
#pragma unroll
                for (int z = 0; z < 2; ++z) {
                    const int l = c8 * 8 + e * 2 + z;
                    const int v = (int)bfround((float)l);
                    const int id = v <= 256 ? v : (v <= 512 ? 128 + (v >> 1) : (v <= 1024 ? 256 + (v >> 2) : 384 + (v >> 3)));
                    kv[z] = kd[id];
                }
                hi[e] = cvtpk(kv[0], kv[1]);
                lo[e] = cvtpk(kv[0] - __uint_as_float(hi[e] << 16), kv[1] - __uint_as_float(hi[e] & 0xffff0000u));
            }
            const int off = (c8 & 3) * 1536 + ((c8 >> 2) + 32) * 16;
            *(u32x4*)(kp + off) = u32x4{hi[0], hi[1], hi[2], hi[3]};
            *(u32x4*)(kp + 6144 + off) = u32x4{lo[0], lo[1], lo[2], lo[3]};
        }
        {
            const int pl = t7 >> 5, rr = t7 & 31;
            *(u32x4*)(kp + pl * 1536 + rr * 16) = u32x4{0, 0, 0, 0};
            *(u32x4*)(kp + 6144 + pl * 1536 + rr * 16) = u32x4{0, 0, 0, 0};
        }
#pragma unroll
        for (int k = 0; k < 8; ++k) {
            const int xx = tid + 256 * k;
#pragma unroll
            for (int c = 0; c < 8; ++c) {
                const int j = (c - xx) & 7;
                const int pos = c * UCOPY + ((xx + j - c + 24) + j) * 2;
                *(unsigned short*)(lds + pos) = (unsigned short)(up[k] & 0xffff);
                *(unsigned short*)(lds + SSM_FAC + pos) = (unsigned short)(up[k] >> 16);
            }
        }
        if (tid < 32) {
            const int xx = tid - 32;
#pragma unroll
            for (int c = 0; c < 8; ++c) {
                const int j = (c - xx) & 7;
                const int e = xx + j - c + 24;
                if (e >= 0) {
                    const int pos = c * UCOPY + (e + j) * 2;
                    *(unsigned short*)(lds + pos) = 0;
                    *(unsigned short*)(lds + SSM_FAC + pos) = 0;
                }
            }
        }
    }
    __syncthreads();
    f32x16 y1 = f32x16{}, y0 = f32x16{};
    {
        const unsigned ub = (unsigned)(uintptr_t)fac + (r32 & 7) * UCOPY + ((r32 >> 3) - hh + 3) * 16;
        const unsigned kb = (unsigned)(uintptr_t)kp + hh * 1536 + (r32 + 32) * 16;
        auto run = [&](f32x16& acc, int a0, int tlo, int thi) {
            for (int t = tlo; t <= thi; t += 2) {
                const int sh = (t + 1) >> 1;
                const unsigned ka = kb + (a0 - sh) * 16;
                const bf16x8 ua = lds_rd128(ub + t * 32), ue = lds_rd128(ub + t * 32 + 32);
                const bf16x8 kh1 = lds_rd128(ka + 3072), kl1 = lds_rd128(ka + 3072 + 6144);
                const bf16x8 kh0 = lds_rd128(ka), kl0 = lds_rd128(ka + 6144);
                acc = MFMA32(ua, kh1, acc); acc = MFMA32(ua, kl1, acc);
                acc = MFMA32(ue, kh0, acc); acc = MFMA32(ue, kl0, acc);
            }
        };
        if (wi == 0) run(y1, 32, -1, 94);
        else { run(y0, 0, -1, 62); run(y1, 32, 95, 126); }
    }
    __syncthreads();
    float* yb = (float*)lds + ch * 2048;
    if (wi == 1) {
#pragma unroll
        for (int r = 0; r < 16; ++r) {
            const int bb = (r & 3) + 8 * (r >> 2) + 4 * hh;
            yb[r32 * 32 + bb] = y0[r];
            yb[1024 + r32 * 32 + bb] = y1[r];
        }
    }
    __syncthreads();
    if (wi == 0) {
#pragma unroll
        for (int r = 0; r < 16; ++r) {
            const int bb = (r & 3) + 8 * (r >> 2) + 4 * hh;
            yb[1024 + r32 * 32 + bb] += y1[r];
        }
    }
    __syncthreads();
    {
        const float ds0 = bf2f(dskip[d0]), ds1 = bf2f(dskip[d0 + 1]);
        const float* ya = (const float*)lds;
#pragma unroll
        for (int k = 0; k < 8; ++k) {
            const int l = tid + 256 * k;
            const float u0 = __uint_as_float(up[k] << 16), u1 = __uint_as_float(up[k] & 0xffff0000u);
            const float o0 = bfround(ya[l]) + bfround(u0 * ds0);
            const float o1 = bfround(ya[2048 + l]) + bfround(u1 * ds1);
            *(unsigned*)(out + (size_t)l * 1024 + 512 + d0) = cvtpk(o0, o1);
        }
    }
}

extern "C" void kernel_launch(void* const* d_in, const int* in_sizes, int n_in, void* d_out, int out_size, void* d_ws,
                              size_t ws_size, hipStream_t stream) {
    (void)in_sizes; (void)n_in; (void)out_size; (void)ws_size;
    const unsigned short* const* in = (const unsigned short* const*)d_in;
    char* ws = (char*)d_ws;
    unsigned short* out = (unsigned short*)d_out;
    proj_kernel<<<256, 512, 0, stream>>>(in[0], in[1], in[2], in[3], in[4], in[5], ws);
    attn_kernel<<<256, 512, 0, stream>>>(ws);
    oproj_kernel<<<256, 256, 0, stream>>>(ws, out);
    ssm_kernel<<<256, 256, 0, stream>>>(in[6], in[7], in[8], in[9], in[10], in[11], in[12], in[13], in[14], out);
}
```

```cpp
#include <hip/hip_runtime.h>
#include <hip/hip_bf16.h>
#include <stdint.h>
#include <type_traits>

typedef __bf16 bf16x8 __attribute__((ext_vector_type(8)));
typedef __bf16 bf16x4 __attribute__((ext_vector_type(4)));
typedef __bf16 bf16x2 __attribute__((ext_vector_type(2)));
typedef short s16x4 __attribute__((ext_vector_type(4)));
typedef float f32x16 __attribute__((ext_vector_type(16)));
typedef float f32x4 __attribute__((ext_vector_type(4)));
typedef float f32x2 __attribute__((ext_vector_type(2)));
typedef unsigned u32x4 __attribute__((ext_vector_type(4)));
typedef unsigned u32x2 __attribute__((ext_vector_type(2)));
#define LAS __attribute__((address_space(3)))
#define DI __device__ __forceinline__
#define MFMA32(a, b, c) __builtin_amdgcn_mfma_f32_32x32x16_bf16((a), (b), (c), 0, 0, 0)

constexpr int SEQ = 2048, NH = 32, HD = 128, DM = 512;
constexpr float QSCALE = 0.08838834764831845f * 1.4426950408889634f;
#define THRV 40.0f
constexpr float THR = THRV;

constexpr size_t WS_Q = 0;
constexpr size_t WS_KV = 16u << 20;
constexpr size_t WS_CTX = 48u << 20;
constexpr size_t WS_FLAG = 72u << 20;
constexpr size_t WS_WO = 64u << 20;

DI unsigned cvtpk(float lo, float hi) { f32x2 v = {lo, hi}; bf16x2 b = __builtin_convertvector(v, bf16x2); return __builtin_bit_cast(unsigned, b); }
DI float bf2f(unsigned short b) { return __uint_as_float(((unsigned)b) << 16); }
DI float bfround(float x) { __bf16 b = (__bf16)x; return (float)b; }
DI unsigned rfl(unsigned x) { return __builtin_amdgcn_readfirstlane(x); }
DI bf16x8 pack8(const f32x16& x, int s, float sc) {
    u32x4 w = {cvtpk(x[8 * s + 0] * sc, x[8 * s + 1] * sc), cvtpk(x[8 * s + 2] * sc, x[8 * s + 3] * sc),
               cvtpk(x[8 * s + 4] * sc, x[8 * s + 5] * sc), cvtpk(x[8 * s + 6] * sc, x[8 * s + 7] * sc)};
    return __builtin_bit_cast(bf16x8, w);
}
DI bf16x8 pack8n(const f32x16& x, int s) {
    u32x4 w = {cvtpk(x[8 * s + 0], x[8 * s + 1]), cvtpk(x[8 * s + 2], x[8 * s + 3]), cvtpk(x[8 * s + 4], x[8 * s + 5]), cvtpk(x[8 * s + 6], x[8 * s + 7])};
    return __builtin_bit_cast(bf16x8, w);
}
DI float swap_max(float x) { auto r = __builtin_amdgcn_permlane32_swap(__float_as_uint(x), __float_as_uint(x), false, false); return fmaxf(__uint_as_float(r[0]), __uint_as_float(r[1])); }
DI float swap_sum(float x) { auto r = __builtin_amdgcn_permlane32_swap(__float_as_uint(x), __float_as_uint(x), false, false); return __uint_as_float(r[0]) + __uint_as_float(r[1]); }
DI bf16x8 lds_rd128(unsigned addr) { return *(const LAS bf16x8*)(uintptr_t)addr; }
#define FRD(dst, addr) dst = lds_rd128(addr)
#define EXP2(x) __builtin_amdgcn_exp2f(x)
DI s16x4 lds_tr(unsigned addr) { return __builtin_amdgcn_ds_read_tr16_b64_v4i16((LAS s16x4*)(uintptr_t)addr); }
DI bf16x8 cat8(s16x4 lo, s16x4 hi) { return __builtin_bit_cast(bf16x8, __builtin_shufflevector(lo, hi, 0, 1, 2, 3, 4, 5, 6, 7)); }

DI void glds1(const void* sbase, unsigned voff, unsigned m0v) {
    unsigned keep;
    asm volatile("s_mov_b32 %0, m0\n\ts_mov_b32 m0, %3\n\ts_nop 0\n\tglobal_load_lds_dwordx4 %1, %2\n\ts_mov_b32 m0, %0"
                 : "=&s"(keep) : "v"(voff), "s"(sbase), "s"(m0v) : "memory");
}
template <int OFF> DI void glds1m(const void* sbase, unsigned voff, unsigned m0v, int left) {
    unsigned keep;
    asm volatile("s_mov_b32 %0, m0\n\ts_mov_b32 m0, %3\n\ts_cmp_gt_i32 %4, 0\n\ts_cselect_b64 exec, -1, 0\n\t"
                 "global_load_lds_dwordx4 %1, %2 offset:%5\n\ts_mov_b64 exec, -1\n\ts_mov_b32 m0, %0"
                 : "=&s"(keep) : "v"(voff), "s"(sbase), "s"(m0v), "s"(left), "i"(OFF) : "memory", "scc");
}
DI void glds4(const void* sbase, unsigned voff, unsigned m0v) {
    unsigned keep;
    asm volatile("s_mov_b32 %0, m0\n\ts_mov_b32 m0, %3\n\ts_nop 0\n\t"
                 "global_load_lds_dwordx4 %1, %2\n\tglobal_load_lds_dwordx4 %1, %2 offset:1024\n\t"
                 "global_load_lds_dwordx4 %1, %2 offset:2048\n\tglobal_load_lds_dwordx4 %1, %2 offset:3072\n\ts_mov_b32 m0, %0"
                 : "=&s"(keep) : "v"(voff), "s"(sbase), "s"(m0v) : "memory");
}
#define WT 1
DI void st16(char* ubase, unsigned voff, bf16x8 v) {
    __amdgpu_buffer_rsrc_t r = __builtin_amdgcn_make_buffer_rsrc(ubase, (short)0, 0x7fffffff, 0x00020000);
    __builtin_amdgcn_raw_buffer_store_b128(__builtin_bit_cast(u32x4, v), r, voff, 0, 16);
}
DI void warm(const void* sbase, unsigned voff, unsigned m0v) {
    unsigned keep;
    asm volatile("s_mov_b32 %0, m0\n\ts_mov_b32 m0, %3\n\ts_nop 0\n\tglobal_load_lds_dword %1, %2\n\ts_mov_b32 m0, %0"
                 : "=&s"(keep) : "v"(voff), "s"(sbase), "s"(m0v) : "memory");
}
#define DG(x)
#define DG1(x)
#define SYNC_ALL() asm volatile("s_waitcnt vmcnt(0) lgkmcnt(0)\n\ts_barrier" ::: "memory")
#define SYNC_LDS() asm volatile("s_waitcnt lgkmcnt(0)\n\ts_barrier" ::: "memory")

#define SBAR() __builtin_amdgcn_sched_barrier(0)
#define PIN(x) asm volatile("" : "+v"(x))
#define PABL_NODMA 0
#define PABL_NOLDS 0
struct PJ {
    const char* xs; const char* w1; const char* w2;
    const char* wp; unsigned winc;
    unsigned lb, xoff0, xoff1, woff, xdst, wdst;
    unsigned xa[4], wa[2];
};
DI void pj_piece(const PJ& c, int q, unsigned buf) {
    if (q < 4) glds1(c.xs + q * 8192, (q & 1) ? c.xoff1 : c.xoff0, c.xdst + buf + q * 1024);
    else if (q < 6) glds1(c.w1 + (q - 4) * 32768, c.woff, c.wdst + buf + (q - 4) * 1024);
    else glds1(c.w2 + (q - 6) * 32768, c.woff, c.wdst + buf + 16384 + (q - 6) * 1024);
}
DI void pj_warm(PJ& c) {
    unsigned keep;
    asm volatile("s_mov_b32 %0, m0\n\ts_mov_b32 m0, %2\n\ts_mov_b64 exec, 0xffffff\n\tglobal_load_lds_dword %1, off\n\ts_mov_b64 exec, -1\n\ts_mov_b32 m0, %0"
                 : "=&s"(keep) : "v"(c.wp), "s"(c.lb + 163840 - 256) : "memory");
    c.wp += c.winc;
}
struct FKV { bf16x8 x[4]; s16x4 k[2], v[2]; };
struct FQ { bf16x8 x[2]; s16x4 a[2], b[2]; };
DI void kv_read(const PJ& c, FKV& f, int j, int s, unsigned buf, unsigned xrow) {
    if (j == 0 || j == 1) f.k[j] = lds_tr(c.lb + buf + 32768 + c.wa[0] + s * 4096 + j * 1024);
    else if (j == 3 || j == 4) f.v[j - 3] = lds_tr(c.lb + buf + 49152 + c.wa[0] + s * 4096 + (j - 3) * 1024);
    else { const int tb = j == 2 ? 0 : j - 4; f.x[tb] = lds_rd128(c.lb + buf + c.xa[s] + xrow + tb * 4096); }
}
DI void q_read(const PJ& c, FQ& f, int j, int s, unsigned buf, unsigned xrow) {
    if (j == 0 || j == 1) f.a[j] = lds_tr(c.lb + buf + 32768 + c.wa[0] + s * 4096 + j * 1024);
    else if (j == 3 || j == 4) f.b[j - 3] = lds_tr(c.lb + buf + 32768 + c.wa[1] + s * 4096 + (j - 3) * 1024);
    else { const int tb = j == 2 ? 0 : 1; f.x[tb] = lds_rd128(c.lb + buf + c.xa[s] + xrow + tb * 4096); }
}
#define SYNC_N(n) asm volatile("s_waitcnt vmcnt(" #n ") lgkmcnt(0)\n\ts_barrier" ::: "memory")
#define MX3(a, b, c) __builtin_fmaxf(__builtin_fmaxf((a), (b)), (c))
DI f32x16 mfma_c(bf16x8 a, bf16x8 b, const f32x16& cc) {
    f32x16 d;
    asm volatile("v_mfma_f32_32x32x16_bf16 %0, %1, %2, %3" : "=&v"(d) : "v"(a), "v"(b), "v"(cc));
    return d;
}
constexpr int NPRE = 6;
struct SB { f32x16 a, b; };
struct AT {
    char* ws; const char* ldp; const char* qh;
    int h, bi, bj, grp, rg, n1, r32, hh;
    unsigned lb, l16;
    unsigned ldoff, ldlim, ldbase;
    unsigned koff, voff;
    int taun, ldleft;
    bf16x8 qf[8], fr[8];
    unsigned pw[16];
    float pe[32];
    f32x16 o[4], negm;
    float mref, lsum;
    DG1(unsigned long long nr; unsigned long long tb; unsigned long long tx; unsigned long long ty; unsigned long long tc;)
};
DI void at_piece(AT& c, int q) {
    const unsigned m0v = c.ldbase + c.ldoff;
    if (q == 0) glds1m<0>(c.ldp, c.l16, m0v, c.ldleft); else if (q == 1) glds1m<1024>(c.ldp, c.l16, m0v, c.ldleft);
    else if (q == 2) glds1m<2048>(c.ldp, c.l16, m0v, c.ldleft); else glds1m<3072>(c.ldp, c.l16, m0v, c.ldleft);
}
DI void at_adv(AT& c) { c.ldp += 32768; c.ldoff += 16384; if (c.ldoff >= c.ldlim) c.ldoff = 0; c.ldleft -= 1; }
DI void at_load_tile(AT& c) { glds4(c.ldp, c.l16, c.ldbase + c.ldoff); at_adv(c); }
DI unsigned kitem(int j) { return ((j & 1) * 8 + (j >> 1)) * 1024; }
DI unsigned vitem(int j) { return ((j & 3) * 4 + (j >> 2)) * 1024; }
DI float& selem(SB& s, int e) { return e < 16 ? *((float*)&s.a + e) : *((float*)&s.b + (e - 16)); }

DI void at_mask(AT& c, SB& s, int tau, int blk) {
    const int row0 = blk * 128 + c.rg * 32;
    if (tau * 64 + 63 > row0) {
        const int dq = row0 + c.r32 - tau * 64 - 4 * c.hh;
#pragma unroll
        for (int r = 0; r < 16; ++r) {
            const int cc = (r & 3) + 8 * (r >> 2);
            if (cc > dq) s.a[r] = -__builtin_inff();
            if (cc + 32 > dq) s.b[r] = -__builtin_inff();
        }
    }
}
DI void at_check(AT& c, SB& s, float ma, bool first) {
    auto pr = __builtin_amdgcn_permlane32_swap(__float_as_uint(ma), __float_as_uint(ma), false, false);
    const float mx = MX3(__uint_as_float(pr[0]), __uint_as_float(pr[1]), ma);
    if (__builtin_expect(first || __any(mx > THR), 0)) {
        const float dl = first ? mx : __builtin_fmaxf(mx, 0.f);
        const float fs = first ? 1.f : __builtin_amdgcn_exp2f(-dl);
        c.mref += dl;
        c.lsum *= fs;
#pragma unroll
        for (int r = 0; r < 16; ++r) { s.a[r] -= dl; s.b[r] -= dl; c.negm[r] = -c.mref; }
        PIN(c.negm);
#pragma unroll
        for (int d = 0; d < 4; ++d)
#pragma unroll
            for (int r = 0; r < 16; ++r) c.o[d][r] *= fs;
        asm volatile("s_nop 1");
        DG(c.nr += 1;)
    }
}
template <bool QK> DI void at_x(AT& c, SB& sc, SB& sn) {
    const unsigned ks_ = c.lb + c.koff + c.l16, vs_ = c.lb + 65536 + c.voff + c.l16;
    DG(unsigned long long t0 = stamp();)
#pragma unroll
    for (int i = 0; i < 16; ++i) {
        const int n = i + NPRE;
        if (n < 16) { if (QK) FRD(c.fr[n & 7], ks_ + kitem(n)); }
        else FRD(c.fr[n & 7], vs_ + vitem(n - 16));
        if (QK) {
            const int s = i >> 1;
            if ((i & 1) == 0) { sn.a = s ? MFMA32(c.fr[i & 7], c.qf[s], sn.a) : mfma_c(c.fr[i & 7], c.qf[s], c.negm); PIN(sn.a); }
            else { sn.b = s ? MFMA32(c.fr[i & 7], c.qf[s], sn.b) : mfma_c(c.fr[i & 7], c.qf[s], c.negm); PIN(sn.b); }
        }
        c.pe[2 * i] = EXP2(i < 8 ? sc.a[2 * i] : sc.b[2 * i - 16]);
        c.pe[2 * i + 1] = EXP2(i < 8 ? sc.a[2 * i + 1] : sc.b[2 * i - 15]);
        if (i > 0) { c.pw[i - 1] = cvtpk(c.pe[2 * i - 2], c.pe[2 * i - 1]); PIN(c.pw[i - 1]); }
        PIN(c.pe[2 * i]); PIN(c.pe[2 * i + 1]);
        SBAR();
    }
    c.pw[15] = cvtpk(c.pe[30], c.pe[31]); PIN(c.pw[15]);
    DG(c.tx += stamp() - t0;)
}
template <bool NEXT, int NLD> DI float at_y(AT& c, SB& sc, SB& sn, int inc, int blkn) {
    DG(unsigned long long t0 = stamp();)
    asm volatile("s_waitcnt vmcnt(0)\n\ts_barrier" ::: "memory");
    DG(unsigned long long t1 = stamp(); c.tb += t1 - t0;)
    if (NEXT) at_mask(c, sn, c.taun, blkn);
    const unsigned vs_ = c.lb + 65536 + c.voff + c.l16;
    c.koff = (c.koff + inc * 16384) & 65535;
    const unsigned ks_ = c.lb + c.koff + c.l16;
    float ps = c.lsum, ma = -__builtin_inff();
#pragma unroll
    for (int i = 0; i < 16; ++i) {
        const int n = i + NPRE;
        if (n < 16) FRD(c.fr[n & 7], vs_ + vitem(n));
        else if (NEXT) FRD(c.fr[n & 7], ks_ + kitem(n - 16));
        c.o[i & 3] = MFMA32(c.fr[i & 7], __builtin_bit_cast(bf16x8, u32x4{c.pw[(i >> 2) * 4], c.pw[(i >> 2) * 4 + 1], c.pw[(i >> 2) * 4 + 2], c.pw[(i >> 2) * 4 + 3]}), c.o[i & 3]);
        ps += c.pe[2 * i]; ps += c.pe[2 * i + 1];
        PIN(ps);
        if (NEXT) {
            if (i < 8) ma = MX3(ma, sn.a[2 * i], sn.a[2 * i + 1]); else ma = MX3(ma, sn.b[2 * i - 16], sn.b[2 * i - 15]);
            PIN(ma);
        }
        if ((i & 1) == 0) {
            const int q = i >> 1;
            if (NLD > (q >> 2)) { at_piece(c, q & 3); if ((q & 3) == 3) at_adv(c); }
        }
        SBAR();
    }
    c.lsum = ps;
    DG(c.ty += stamp() - t1;)
    return ma;
}
DI void at_vadv(AT& c, int inc) { c.voff += inc * 16384; if (c.voff >= 98304) c.voff -= 98304; c.taun += 0; }
template <int KIND, int NLD> DI void at_step(AT& c, SB& sc, SB& sn, int incv, int inck, int blkn) {
    at_x<true>(c, sc, sn);
    if constexpr (KIND == 1) {
#pragma unroll
        for (int s = 0; s < 8; ++s) c.qf[s] = *(const bf16x8*)(c.qh + c.rg * 8192 + s * 1024 + c.l16);
    }
    const float ma = at_y<true, NLD>(c, sc, sn, inck, blkn);
    at_vadv(c, incv);
    c.taun += inck;
    DG(unsigned long long t0 = stamp();)
    if constexpr (KIND == 2) {
        if (!c.grp) {
            const float inv = 1.0f / swap_sum(c.lsum);
            char* dst = c.ws + WS_CTX + ((size_t)((c.bi * 4 + c.rg) * 256 + c.h * 8) << 10) + c.l16;
#pragma unroll
            for (int db = 0; db < 4; ++db)
#pragma unroll
                for (int sp = 0; sp < 2; ++sp) *(bf16x8*)(dst + (db * 2 + sp) * 1024) = pack8(c.o[db], sp, inv);
#pragma unroll
            for (int k = 0; k < 4; ++k) c.o[k] = f32x16{};
            c.mref = 0.f; c.lsum = 0.f;
        }
        at_check(c, sn, ma, !c.grp);
    } else {
        at_check(c, sn, ma, false);
    }
    if constexpr (KIND == 1) {
#pragma unroll
        for (int s = 0; s < 8; ++s) PIN(c.qf[s]);
        if (!c.grp) { c.negm = f32x16{}; PIN(c.negm); asm volatile("s_nop 1"); }
    }
    DG(c.tc += stamp() - t0;)
}
typedef __attribute__((address_space(1))) unsigned gu32;
__global__ __launch_bounds__(512, 2) void attn_kernel(const unsigned short* __restrict__ x, const unsigned short* __restrict__ ckv,
                                                      const unsigned short* __restrict__ wq, const unsigned short* __restrict__ wuk,
                                                      const unsigned short* __restrict__ wuv, const unsigned short* __restrict__ wo,
                                                      char* __restrict__ ws) {
    __shared__ __attribute__((aligned(16))) char lds[163840];
    const int tid = threadIdx.x, lane = tid & 63, w = rfl(tid >> 6), r32 = lane & 31, hh = lane >> 5;
    const int b = blockIdx.x, xcd = b & 7, idx = b >> 3, h = xcd * 4 + (idx & 3), rb = idx >> 2;
    const int bi = rb, bj = 15 - rb, grp = w >> 2, rg = w & 3;
    const unsigned l16 = lane * 16;
    gu32* kvflag = (gu32*)(ws + WS_FLAG) + h * 32;
    DG1(unsigned long long* dbp = (unsigned long long*)(ws + (130u << 20)) + (b * 8 + w) * 8; const unsigned long long T0 = stamp(); const unsigned long long R0 = rstamp();)
#define PST(i) DG1({ unsigned long long tt = stamp() - T0; if (lane == 0) dbp[i] = tt; })
    AT c;
    c.ws = ws; c.h = h; c.bi = bi; c.bj = bj; c.grp = grp; c.rg = rg;
    c.n1 = 2 * (bi + 1); c.r32 = r32; c.hh = hh;
    c.lb = (unsigned)(uintptr_t)lds; c.l16 = l16;
    c.ldp = ws + WS_KV + ((size_t)h << 20) + w * 4096;
    c.qh = ws + WS_Q + ((size_t)b << 15);
    c.ldbase = c.lb + grp * 65536 + rg * 4096;
    c.ldoff = grp ? 49152 : 0; c.ldlim = grp ? 98304 : 65536; c.koff = 16384; c.voff = 49152; c.taun = 1; c.ldleft = 32 - 2 * bi;
    {
    PJ p;
    p.lb = c.lb;
    {
        const int r8 = lane >> 3, sl8 = lane & 7, r4 = lane >> 4, sl16 = lane & 15;
        p.xoff0 = r8 * 1024 + ((sl8 ^ (r8 >> 1)) << 4);
        p.xoff1 = r8 * 1024 + ((sl8 ^ (4 + (r8 >> 1))) << 4);
        p.woff = r4 * 8192 + ((sl16 ^ (r4 << 2)) << 4);
    }
    p.xdst = p.lb + w * 4096; p.wdst = p.lb + 32768 + w * 2048;
    const size_t wrow_off = (size_t)(w * 8) * 8192 + h * 256;
    p.xs = (const char*)ckv + (size_t)(rb * 256 + w * 32) * 1024; p.w1 = (const char*)wuk + wrow_off; p.w2 = (const char*)wuv + wrow_off;
#pragma unroll
    for (int q = 0; q < 8; ++q) pj_piece(p, q, 0);
    p.xs += 128; p.w1 += 64 * 8192; p.w2 += 64 * 8192;
#pragma unroll
    for (int q = 0; q < 8; ++q) pj_piece(p, q, 65536);
    p.xs += 128; p.w1 += 64 * 8192; p.w2 += 64 * 8192;
    const int sh = idx * 8 + w;
    const size_t warm_x = (size_t)(sh * 8 + (lane & 7)) * 1024 + ((lane >> 3) & 1) * 128;
    const size_t warm_w = (size_t)(sh >> 1) * 8192 + xcd * 1024 + ((sh & 1) * 4 + (lane & 3)) * 128;
    p.winc = lane < 16 ? 256u : 128u * 8192u;
    p.wp = (lane < 16 ? (const char*)ckv + warm_x : (lane < 20 ? (const char*)wuk : (const char*)wuv) + warm_w) + (size_t)p.winc;
    pj_warm(p);
#pragma unroll
    for (int q = 0; q < 2; ++q) glds1((const char*)wo + (size_t)(b * 16 + w * 2 + q) * 1024, l16, p.lb + 131072 + (w * 2 + q) * 1024);
    {
        const int f = (r32 >> 1) & 7;
#pragma unroll
        for (int s = 0; s < 4; ++s) p.xa[s] = r32 * 128 + (((2 * s + hh) ^ f) << 4);
    }
    const int g16 = (lane >> 4) & 1, q4 = (lane & 15) >> 2, p4 = lane & 3;
#define WBASE(dq) ((8 * hh + q4) * 256 + ((((dq) ^ q4) * 4 + g16 * 2 + (p4 >> 1)) << 4) + (p4 & 1) * 8)
    const int th = w >> 2, dq = w & 3;
    const int tq = w >> 1, dh = w & 1;
    p.wa[0] = WBASE(dq);
    SYNC_N(11);
    PST(2)
    FQ g0, g1;
    {
        const unsigned xrow = th * 16384;
        f32x16 acck[4], accv[4];
#pragma unroll
        for (int i = 0; i < 4; ++i) { acck[i] = f32x16{}; accv[i] = f32x16{}; }
        FKV f0, f1;
#pragma unroll
        for (int j = 0; j < 8; ++j) kv_read(p, f0, j, 0, 0, xrow);
#define KV_STEP(fc, fn, sn, bn, RD, NQ, bd)                                                                     \
        {                                                                                                       \
            const bf16x8 wk = cat8(fc.k[0], fc.k[1]), wv = cat8(fc.v[0], fc.v[1]);                              \
            _Pragma("unroll") for (int j = 0; j < 8; ++j) {                                                     \
                if (RD) kv_read(p, fn, j, sn, bn, xrow);                                                        \
                if (j & 1) { accv[j >> 1] = MFMA32(fc.x[j >> 1], wv, accv[j >> 1]); PIN(accv[j >> 1]); }        \
                else { acck[j >> 1] = MFMA32(wk, fc.x[j >> 1], acck[j >> 1]); PIN(acck[j >> 1]); }              \
                if (j < NQ) pj_piece(p, j, bd);                                                                 \
                SBAR();                                                                                         \
            }                                                                                                   \
        }
#define KV_TILE(buf, RD, NQ, SYNC, WARM)                                                                        \
        KV_STEP(f0, f1, 1, buf, true, 0, 0) KV_STEP(f1, f0, 2, buf, true, 0, 0) KV_STEP(f0, f1, 3, buf, true, 0, 0) \
        SYNC;                                               \
        KV_STEP(f1, f0, 0, (buf) ^ 65536u, RD, NQ, buf)                                                         \
        if (WARM) pj_warm(p);                                                                                   \
        p.xs += 128; p.w1 += 64 * 8192; p.w2 += 64 * 8192;
        KV_TILE(0u, true, 8, SYNC_N(3), true)
#pragma unroll 1
        for (int t = 1; t < 6; ++t) {
            const unsigned buf = (t & 1) << 16;
            if (t == 2) p.wp = lane < 16 ? (const char*)x + warm_x : (const char*)wq + warm_w;
            KV_TILE(buf, true, 8, SYNC_N(1), true)
        }
#pragma unroll
        for (int it = 0; it < 2; ++it) {
            const int nb = w + it * 8;
            const unsigned a = p.lb + 131072 + (4 * hh + q4) * 1024 + nb * 64 + g16 * 32 + p4 * 8;
            const bf16x8 v = cat8(lds_tr(a), lds_tr(a + 8192));
            *(bf16x8*)(ws + WS_WO + ((size_t)(nb * 256 + b) * 64 + lane) * 16) = v;
        }
        SBAR();
        p.xs = (const char*)x + (size_t)((grp ? bj : bi) * 128 + rg * 32) * 1024; p.w1 = (const char*)wq + wrow_off;
        KV_TILE(0u, true, 6, SYNC_N(3), false)
        KV_TILE(65536u, false, 6, SYNC_N(0), false)
#pragma unroll
        for (int q = 0; q < 6; ++q) pj_piece(p, q, 114688u);
        p.xs += 128; p.w1 += 64 * 8192;
        PST(3)
        p.wa[0] = WBASE(dh * 2); p.wa[1] = WBASE(dh * 2 + 1);
#pragma unroll
        for (int j = 0; j < 6; ++j) q_read(p, g0, j, 0, 0, tq * 8192);
#pragma unroll
        for (int tb = 0; tb < 4; ++tb) {
            const int kb32 = rb * 8 + th * 4 + tb;
            char* tile = ws + WS_KV + ((size_t)(h * 32 + (kb32 >> 1)) << 15);
#pragma unroll
            for (int sp = 0; sp < 2; ++sp) {
                st16(tile + (((kb32 & 1) * 8 + dq * 2 + sp) << 10), l16, pack8n(acck[tb], sp));
                st16(tile + 16384 + ((dq * 4 + (kb32 & 1) * 2 + sp) << 10), l16, pack8n(accv[tb], sp));
            }
        }
    }
    PST(4)
    {
        const unsigned xrow = tq * 8192;
        f32x16 acc[4];
#pragma unroll
        for (int i = 0; i < 4; ++i) acc[i] = f32x16{};
#define Q_STEP(fc, fn, sn, bn, RD, DMA, bd)                                                                     \
        {                                                                                                       \
            const bf16x8 w0 = cat8(fc.a[0], fc.a[1]), w1 = cat8(fc.b[0], fc.b[1]);                              \
            _Pragma("unroll") for (int j = 0; j < 4; ++j) {                                                     \
                if (RD) { q_read(p, fn, j < 2 ? 2 * j : j + 2, sn, bn, xrow); if (j < 2) q_read(p, fn, 2 * j + 1, sn, bn, xrow); } \
                acc[j] = MFMA32((j & 1) ? w1 : w0, fc.x[j >> 1], acc[j]); PIN(acc[j]);                          \
                if (DMA) { pj_piece(p, j, bd); if (j < 2) pj_piece(p, 4 + j, bd); }                             \
                SBAR();                                                                                         \
            }                                                                                                   \
        }
#define Q_TILE3(buf) Q_STEP(g0, g1, 1, buf, true, false, 0) Q_STEP(g1, g0, 2, buf, true, false, 0) Q_STEP(g0, g1, 3, buf, true, false, 0)
#define Q_LAST(buf, nbuf, DMA) Q_STEP(g1, g0, 0, nbuf, true, DMA, buf) p.xs += 128; p.w1 += 64 * 8192;
        constexpr unsigned QB0 = 0u, QB1 = 65536u, QB2 = 114688u;
        Q_TILE3(QB0) SYNC_N(22); Q_LAST(QB0, QB1, true)
        Q_TILE3(QB1) SYNC_N(22); Q_LAST(QB1, QB2, true)
        Q_TILE3(QB2) SYNC_N(6);
        if (tid == 0) __hip_atomic_fetch_add(kvflag, 1u, __ATOMIC_RELAXED, __HIP_MEMORY_SCOPE_AGENT);
        Q_LAST(QB2, QB0, true)
        Q_TILE3(QB0) SYNC_N(6); Q_LAST(QB0, QB1, true)
        Q_TILE3(QB1) SYNC_N(6); Q_LAST(QB1, QB2, true)
        Q_TILE3(QB2)
        if (w == 0) {
            unsigned spins = 0;
            while (rfl(__hip_atomic_load(kvflag, __ATOMIC_RELAXED, __HIP_MEMORY_SCOPE_AGENT)) < 8u && ++spins < (1u << 22)) __builtin_amdgcn_s_sleep(2);
            __builtin_amdgcn_fence(__ATOMIC_ACQUIRE, "agent");
        }
        SYNC_N(6); Q_LAST(QB2, QB0, false)
        Q_TILE3(QB0) SYNC_N(0);
        at_load_tile(c);
        if (!grp) at_load_tile(c);
        Q_LAST(QB0, QB1, false)
        Q_TILE3(QB1)
        Q_STEP(g1, g0, 0, 0u, false, false, 0)
        PST(5)
#pragma unroll
        for (int tb = 0; tb < 2; ++tb) {
            const int tk = tq * 2 + tb;
            char* xq = lds + (tk < 4 ? 32768 + tk * 8192 : 131072 + (tk - 4) * 8192) + l16;
#pragma unroll
            for (int db = 0; db < 2; ++db)
#pragma unroll
                for (int sp = 0; sp < 2; ++sp) {
                    const int sfr = (dh * 2 + db) * 2 + sp;
                    const bf16x8 v = pack8(acc[tb * 2 + db], sp, QSCALE);
                    *(bf16x8*)(xq + sfr * 1024) = v;
                    if (tq >= 2) *(bf16x8*)(ws + WS_Q + ((size_t)b << 15) + (tk - 4) * 8192 + sfr * 1024 + l16) = v;
                }
        }
    }
    }
    const int n1 = c.n1;
    const int blk1 = grp ? bj : bi;
    DG1(c.nr = c.tb = c.tx = c.ty = c.tc = 0;)
    SYNC_ALL();
#pragma unroll
    for (int s = 0; s < 8; ++s) c.qf[s] = lds_rd128(c.lb + (grp ? 131072 : 32768) + rg * 8192 + s * 1024 + l16);
    c.negm = f32x16{}; PIN(c.negm);
#pragma unroll
    for (int i = 0; i < 4; ++i) c.o[i] = f32x16{};
    c.mref = 0.f; c.lsum = 0.f;
    SYNC_LDS();
    at_load_tile(c);
    if (!grp) at_load_tile(c);
    SB sA, sB;
    {
        const unsigned ks_ = c.lb + l16;
#pragma unroll
        for (int s = 0; s < 8; ++s) {
            const bf16x8 k0 = lds_rd128(ks_ + s * 1024), k1 = lds_rd128(ks_ + 8192 + s * 1024);
            sA.a = MFMA32(k0, c.qf[s], s ? sA.a : f32x16{});
            sA.b = MFMA32(k1, c.qf[s], s ? sA.b : f32x16{});
        }
#pragma unroll
        for (int i = 0; i < 8; ++i) c.fr[i] = lds_rd128(ks_ + 16384 + kitem(i));
        at_mask(c, sA, 0, blk1);
        float ma = MX3(sA.a[0], sA.a[1], sA.b[0]);
        ma = __builtin_fmaxf(ma, sA.b[1]);
#pragma unroll
        for (int r = 2; r < 16; r += 2) { ma = MX3(ma, sA.a[r], sA.a[r + 1]); ma = MX3(ma, sA.b[r], sA.b[r + 1]); }
        at_check(c, sA, ma, true);
    }
    for (int t = 0; t < n1 - 2; t += 2) {
        at_step<0, 1>(c, sA, sB, 1, 1, blk1);
        at_step<0, 1>(c, sB, sA, 1, 1, blk1);
    }
    at_step<1, 2>(c, sA, sB, 1, 1 + grp, blk1);
    at_step<2, 2>(c, sB, sA, 1 + grp, 2, bj);
    for (int t = n1; t < 16; t += 2) {
        at_step<0, 2>(c, sA, sB, 2, 2, bj);
        at_step<0, 2>(c, sB, sA, 2, 2, bj);
    }
    at_x<false>(c, sA, sB);
    at_y<false, 0>(c, sA, sB, 0, bj);
    f32x16 (&o)[4] = c.o;
    float lsum = c.lsum;
    const float mref = c.mref;
    lsum = swap_sum(lsum);
    SYNC_ALL();
    {
        char* mine = lds + w * 9216;
#pragma unroll
        for (int d = 0; d < 2; ++d)
#pragma unroll
            for (int g = 0; g < 4; ++g) {
                const f32x16& src = grp ? o[d] : o[2 + d];
                f32x4 v = {src[4 * g], src[4 * g + 1], src[4 * g + 2], src[4 * g + 3]};
                *(f32x4*)(mine + (d * 4 + g) * 1024 + l16) = v;
            }
        *(f32x2*)(mine + 8192 + lane * 8) = f32x2{mref, lsum};
    }
    SYNC_LDS();
    {
        const char* oth = lds + (w ^ 4) * 9216;
        const f32x2 ml = *(const f32x2*)(oth + 8192 + lane * 8);
        const float m = fmaxf(mref, ml[0]);
        const float fa = __builtin_amdgcn_exp2f(mref - m), fb = __builtin_amdgcn_exp2f(ml[0] - m);
        const float inv = 1.0f / (lsum * fa + ml[1] * fb);
        const float ca = fa * inv, cb = fb * inv;
        char* dst = ws + WS_CTX + ((size_t)((bj * 4 + rg) * 256 + h * 8 + (grp ? 4 : 0)) << 10) + l16;
#pragma unroll
        for (int d = 0; d < 2; ++d) {
            f32x16 acc = grp ? o[2 + d] : o[d];
#pragma unroll
            for (int g = 0; g < 4; ++g) {
                const f32x4 v = *(const f32x4*)(oth + (d * 4 + g) * 1024 + l16);
#pragma unroll
                for (int e = 0; e < 4; ++e) acc[4 * g + e] = acc[4 * g + e] * ca + v[e] * cb;
            }
#pragma unroll
            for (int sp = 0; sp < 2; ++sp) *(bf16x8*)(dst + (d * 2 + sp) * 1024) = pack8n(acc, sp);
        }
    }
    DG1(asm volatile("s_waitcnt vmcnt(0)" ::: "memory");)
    DG1(if (lane == 0) { unsigned long long* dbg = (unsigned long long*)(ws + (128u << 20)) + (b * 8 + w) * 8;
        dbg[0] = stamp() - T0; dbg[1] = rstamp() - R0; dbg[2] = c.nr * 1000; dbg[3] = c.tb; dbg[4] = c.tx; dbg[5] = c.ty; dbg[6] = c.tc; dbg[7] = R0; })
}

struct cf { float r, i; };
DI cf cmul(cf a, cf b) { return cf{a.r * b.r - a.i * b.i, a.r * b.i + a.i * b.r}; }
DI cf cexp_scaled(float a2, float brev, float v) {
    const float e = __builtin_amdgcn_exp2f(a2 * v);
    const float ph = __builtin_amdgcn_fractf(brev * v);
    return cf{e * __builtin_amdgcn_cosf(ph), e * __builtin_amdgcn_sinf(ph)};
}
constexpr int GROW = 528;
constexpr int UCOPY = 4160;
constexpr int T_RED = 0;
constexpr int T_UC = 49152;
constexpr int T_KP = T_UC + 2 * 8 * UCOPY;
constexpr int T_KD = T_KP + 2 * 12288;
constexpr int T_UL = T_KD + 4 * 2576;
constexpr int T_CNT = T_UL + 2 * 4224;
constexpr int T_END = T_CNT + 16;
static_assert(T_END <= 163840, "lds");
DI void role_sync(LAS unsigned* cnt, unsigned target, int lane) {
    asm volatile("s_waitcnt lgkmcnt(0)" ::: "memory");
    if (lane == 0) __hip_atomic_fetch_add(cnt, 1u, __ATOMIC_RELAXED, __HIP_MEMORY_SCOPE_WORKGROUP);
    while (__hip_atomic_load(cnt, __ATOMIC_RELAXED, __HIP_MEMORY_SCOPE_WORKGROUP) < target) __builtin_amdgcn_s_sleep(1);
    asm volatile("" ::: "memory");
}
constexpr int OD = 8;
__global__ __launch_bounds__(512, 2) void tail_kernel(char* __restrict__ ws, unsigned short* __restrict__ out,
                                                      const unsigned short* __restrict__ u, const unsigned short* __restrict__ are,
                                                      const unsigned short* __restrict__ aim, const unsigned short* __restrict__ bre,
                                                      const unsigned short* __restrict__ bim, const unsigned short* __restrict__ cre,
                                                      const unsigned short* __restrict__ cim, const unsigned short* __restrict__ logdt,
                                                      const unsigned short* __restrict__ dskip) {
    __shared__ __attribute__((aligned(16))) char lds[T_END];
    const int tid = threadIdx.x, lane = tid & 63, w8 = rfl(tid >> 6), r32 = lane & 31, hh = lane >> 5;
    const int b = blockIdx.x, xcd = b & 7, idx = b >> 3;
    LAS unsigned* cnts = (LAS unsigned*)(uintptr_t)((unsigned)(uintptr_t)lds + T_CNT);
    if (tid < 2) cnts[tid] = 0;
    const int d0 = xcd * 64 + idx * 2;
    unsigned up[8];
    unsigned prm[7];
    if (w8 >= 4) {
        const int d = d0 + ((w8 - 4) >> 1), o = d * 128 + (w8 & 1) * 64 + lane;
        prm[0] = logdt[d]; prm[1] = are[o]; prm[2] = aim[o]; prm[3] = bre[o]; prm[4] = bim[o]; prm[5] = cre[o]; prm[6] = cim[o];
#pragma unroll
        for (int k = 0; k < 8; ++k) up[k] = *(const unsigned*)(u + (size_t)(tid - 256 + 256 * k) * DM + d0);
    }
    asm volatile("s_waitcnt lgkmcnt(0)\n\ts_barrier" ::: "memory");
    DG1(unsigned long long* dbg = (unsigned long long*)(ws + (129u << 20)) + (b * 8 + w8) * 8; const unsigned long long T0 = stamp(); const unsigned long long R0 = rstamp();)
#define TST(i) DG1({ unsigned long long tt = stamp() - T0; if (lane == 0) dbg[i] = tt; })
    if (w8 < 4) {
        const int w = w8, mb = xcd * 4 + (idx & 3), nb = idx >> 2;
        const char* a0 = ws + WS_CTX + ((size_t)((mb * 2) * 256 + w * 64) << 10) + lane * 16;
        const char* b0 = ws + WS_WO + ((size_t)((nb * 2) * 256 + w * 64) << 10) + lane * 16;
        f32x16 acc[4];
#pragma unroll
        for (int i = 0; i < 4; ++i) acc[i] = f32x16{};
        bf16x8 rg[OD][4];
#pragma unroll
        for (int k = 0; k < OD; ++k) {
            rg[k][0] = *(const bf16x8*)(a0 + k * 1024); rg[k][1] = *(const bf16x8*)(b0 + k * 1024);
            rg[k][2] = *(const bf16x8*)(a0 + 262144 + k * 1024); rg[k][3] = *(const bf16x8*)(b0 + 262144 + k * 1024);
        }
        SBAR();
        for (int k0 = 0; k0 < 64 - OD; k0 += OD) {
#pragma unroll
            for (int k = 0; k < OD; ++k) {
                const bf16x8 c0 = rg[k][0], w0 = rg[k][1], c1 = rg[k][2], w1 = rg[k][3];
                acc[0] = MFMA32(w0, c0, acc[0]);
                acc[1] = MFMA32(w1, c0, acc[1]);
                acc[2] = MFMA32(w0, c1, acc[2]);
                acc[3] = MFMA32(w1, c1, acc[3]);
                SBAR();
                const int kn = (k0 + OD + k) * 1024;
                rg[k][0] = *(const bf16x8*)(a0 + kn); rg[k][1] = *(const bf16x8*)(b0 + kn);
                rg[k][2] = *(const bf16x8*)(a0 + 262144 + kn); rg[k][3] = *(const bf16x8*)(b0 + 262144 + kn);
                SBAR();
            }
        }
#pragma unroll
        for (int k = 0; k < OD; ++k) {
            acc[0] = MFMA32(rg[k][1], rg[k][0], acc[0]);
            acc[1] = MFMA32(rg[k][3], rg[k][0], acc[1]);
            acc[2] = MFMA32(rg[k][1], rg[k][2], acc[2]);
            acc[3] = MFMA32(rg[k][3], rg[k][2], acc[3]);
        }
        TST(2)
#pragma unroll
        for (int i = 0; i < 4; ++i) {
            if (i != w) {
                const int slot = w < i ? w : w - 1;
#pragma unroll
                for (int g = 0; g < 4; ++g)
                    *(f32x4*)(lds + T_RED + (i * 3 + slot) * 4096 + g * 1024 + lane * 16) = f32x4{acc[i][4 * g], acc[i][4 * g + 1], acc[i][4 * g + 2], acc[i][4 * g + 3]};
            }
        }
        role_sync(cnts, 4, lane);
        const f32x16 own = w == 0 ? acc[0] : (w == 1 ? acc[1] : (w == 2 ? acc[2] : acc[3]));
        unsigned pk[8];
#pragma unroll
        for (int g = 0; g < 4; ++g) {
            f32x4 sm = f32x4{own[4 * g], own[4 * g + 1], own[4 * g + 2], own[4 * g + 3]};
#pragma unroll
            for (int sl = 0; sl < 3; ++sl) sm += *(const f32x4*)(lds + T_RED + (w * 3 + sl) * 4096 + g * 1024 + lane * 16);
            pk[2 * g] = cvtpk(sm[0], sm[1]); pk[2 * g + 1] = cvtpk(sm[2], sm[3]);
        }
        const int q = mb * 64 + (w >> 1) * 32 + r32;
        unsigned short* orow = out + (size_t)q * 1024 + nb * 64 + (w & 1) * 32;
#pragma unroll
        for (int gp = 0; gp < 2; ++gp) {
            auto r0 = __builtin_amdgcn_permlane32_swap(pk[4 * gp + 0], pk[4 * gp + 2], false, false);
            auto r1 = __builtin_amdgcn_permlane32_swap(pk[4 * gp + 1], pk[4 * gp + 3], false, false);
            *(u32x4*)(orow + gp * 16 + hh * 8) = u32x4{r0[0], r1[0], r0[1], r1[1]};
        }
        DG1(asm volatile("s_waitcnt vmcnt(0)" ::: "memory");)
        TST(0)
        DG1({ unsigned long long tt = rstamp() - R0; if (lane == 0) { dbg[1] = tt; dbg[7] = R0; } })
        return;
    }
    const int w = w8 - 4, t8 = tid - 256, ch = w >> 1, wi = w & 1, t7 = t8 & 127;
    LAS unsigned* cnt = cnts + 1;
#pragma unroll
    for (int i = 0; i < 7; ++i) PIN(prm[i]);
    {
#define PF(i) __uint_as_float(prm[i] << 16)
        const float dt = bfround(expf(PF(0)));
        const float a = dt * PF(1), bb = dt * PF(2);
        const cf dB = cf{dt * PF(3), dt * PF(4)};
        const cf coef = cmul(cf{PF(5), PF(6)}, dB);
        const float a2 = a * 1.4426950408889634f, brev = bb * 0.15915494309189535f;
        char* scr = lds + T_UC + w * (16 * GROW);
        const int i16 = lane & 15, g4 = lane >> 4;
        const char* frd = scr + i16 * GROW + g4 * 16;
        {
            const cf e8 = cexp_scaled(a2, brev, 8.f);
            cf fcur = cf{1.f, 0.f};
#pragma unroll
            for (int j = 0; j < 16; ++j) {
                *(f32x2*)(scr + j * GROW + lane * 8) = f32x2{fcur.r, fcur.i};
                fcur = (j == 7) ? cexp_scaled(a2, brev, 64.f) : cmul(fcur, e8);
            }
        }
        f32x4 fb[8];
#pragma unroll
        for (int kk = 0; kk < 8; ++kk) fb[kk] = *(const f32x4*)(frd + kk * 64);
        cf lo[8];
        lo[0] = cf{1.f, 0.f}; lo[1] = cexp_scaled(a2, brev, 1.f);
#pragma unroll
        for (int c = 2; c < 8; ++c) lo[c] = cmul(lo[c - 1], lo[1]);
        const cf e128 = cexp_scaled(a2, brev, 128.f);
        cf hcur = coef;
        float* kd = (float*)(lds + T_KD + w * 2576);
#pragma unroll
        for (int tile = 0; tile < 3; ++tile) {
            const int klo = tile == 0 ? 0 : (tile == 1 ? 2 : 8), khi = tile == 0 ? 2 : (tile == 1 ? 8 : 17);
            int row = 0;
#pragma unroll
            for (int k = klo; k < khi; ++k) {
                const int cnt_ = k < 2 ? 8 : (k < 4 ? 4 : (k < 8 ? 2 : 1));
                const int stp = 8 / cnt_;
#pragma unroll
                for (int c = 0; c < cnt_; ++c) {
                    const cf g = c ? cmul(hcur, lo[c * stp]) : hcur;
                    *(f32x2*)(scr + row * GROW + lane * 8) = f32x2{g.r, -g.i};
                    ++row;
                }
                hcur = ((k & 3) == 3) ? cmul(coef, cexp_scaled(a2, brev, 128.f * (k + 1))) : cmul(hcur, e128);
            }
            f32x4 acc = f32x4{}, acc2 = f32x4{};
#pragma unroll
            for (int kk = 0; kk < 8; ++kk) {
                const f32x4 ga = *(const f32x4*)(frd + kk * 64);
                acc = __builtin_amdgcn_mfma_f32_16x16x4f32(ga[0], fb[kk][0], acc, 0, 0, 0);
                acc2 = __builtin_amdgcn_mfma_f32_16x16x4f32(ga[1], fb[kk][1], acc2, 0, 0, 0);
                acc = __builtin_amdgcn_mfma_f32_16x16x4f32(ga[2], fb[kk][2], acc, 0, 0, 0);
                acc2 = __builtin_amdgcn_mfma_f32_16x16x4f32(ga[3], fb[kk][3], acc2, 0, 0, 0);
            }
#pragma unroll
            for (int e = 0; e < 4; ++e) {
                const int i = g4 * 4 + e, j = i16;
                int id;
                if (tile == 0) id = (i >> 3) * 128 + (i & 7) + 8 * j;
                else if (tile == 1) id = i < 8 ? 256 + (i >> 2) * 64 + (i & 3) + 4 * j : 384 + ((i - 8) >> 1) * 32 + (i & 1) + 2 * j;
                else id = 512 + i * 16 + j;
                if (tile < 2 || id <= 640) kd[id] = acc[e] + acc2[e];
            }
        }
    }
    TST(2)
    {
        unsigned short* w0 = (unsigned short*)(lds + T_UL), * w1 = (unsigned short*)(lds + T_UL + 4224);
#pragma unroll
        for (int k = 0; k < 8; ++k) {
            const int m = 2055 - (t8 + 256 * k);
            w0[m] = (unsigned short)(up[k] & 0xffff);
            w1[m] = (unsigned short)(up[k] >> 16);
        }
        if (t8 < 2) *(u32x4*)(lds + T_UL + t8 * 4224 + 257 * 16) = u32x4{0, 0, 0, 0};
    }
    role_sync(cnt, 4, lane);
    TST(3)
    char* kp = lds + T_KP + ch * 12288;
    char* uc = lds + T_UC + ch * (8 * UCOPY);
    {
        const float* kd0 = (const float*)(lds + T_KD + (ch * 2) * 2576), * kd1 = (const float*)(lds + T_KD + (ch * 2 + 1) * 2576);
#pragma unroll
        for (int it = 0; it < 2; ++it) {
            const int c8 = t7 + it * 128;
            unsigned hi[4], lo[4];
#pragma unroll
            for (int e = 0; e < 4; ++e) {
                float kv[2];
#pragma unroll
                for (int z = 0; z < 2; ++z) {
                    const int l = c8 * 8 + e * 2 + z;
                    const int v = (int)bfround((float)l);
                    const int id = v <= 256 ? v : (v <= 512 ? 128 + (v >> 1) : (v <= 1024 ? 256 + (v >> 2) : 384 + (v >> 3)));
                    kv[z] = kd0[id] + kd1[id];
                }
                hi[e] = cvtpk(kv[0], kv[1]);
                lo[e] = cvtpk(kv[0] - __uint_as_float(hi[e] << 16), kv[1] - __uint_as_float(hi[e] & 0xffff0000u));
            }
            const int off = (c8 & 3) * 1536 + ((c8 >> 2) + 32) * 16;
            *(u32x4*)(kp + off) = u32x4{hi[0], hi[1], hi[2], hi[3]};
            *(u32x4*)(kp + 6144 + off) = u32x4{lo[0], lo[1], lo[2], lo[3]};
        }
        {
            const int pl = t7 >> 5, rr = t7 & 31;
            *(u32x4*)(kp + pl * 1536 + rr * 16) = u32x4{0, 0, 0, 0};
            *(u32x4*)(kp + 6144 + pl * 1536 + rr * 16) = u32x4{0, 0, 0, 0};
        }
        const char* ul = lds + T_UL + ch * 4224;
#pragma unroll
        for (int it = 0; it < 2; ++it) {
            const int rho = 1 + t7 + it * 128;
            const u32x4 x0 = *(const u32x4*)(ul + rho * 16), x1 = *(const u32x4*)(ul + rho * 16 + 16);
            const unsigned dd[8] = {x0[0], x0[1], x0[2], x0[3], x1[0], x1[1], x1[2], x1[3]};
#pragma unroll
            for (int c = 0; c < 8; ++c) {
                const int e = 7 - c;
                u32x4 v;
#pragma unroll
                for (int k = 0; k < 4; ++k)
                    v[k] = (e & 1) ? __builtin_amdgcn_alignbit(dd[(e + 1) / 2 + k], dd[(e - 1) / 2 + k], 16) : dd[e / 2 + k];
                *(u32x4*)(uc + c * UCOPY + (259 - rho) * 16) = v;
            }
        }
        if (t7 < 24) *(u32x4*)(uc + (t7 & 7) * UCOPY + (t7 >> 3) * 16) = u32x4{0, 0, 0, 0};
    }
    role_sync(cnt, 8, lane);
    TST(4)
    f32x16 y0 = f32x16{}, y1 = f32x16{}, y2 = f32x16{};
    {
        const int tb = -1 + 32 * wi;
        const unsigned ub = (unsigned)(uintptr_t)uc + (r32 & 7) * UCOPY + ((r32 >> 3) - hh + 3) * 16 + tb * 32;
        const unsigned kb = (unsigned)(uintptr_t)kp + hh * 1536 + (r32 + 32 - 16 * wi - 15) * 16;
#pragma unroll
        for (int pr = 0; pr < 16; ++pr) {
            const unsigned ka = kb + (15 - pr) * 16;
#pragma unroll
            for (int z = 0; z < 2; ++z) {
                const unsigned ko = ka + (z ? 0 : 3072);
                const bf16x8 ua = lds_rd128(ub + (2 * pr + z) * 32), ub2 = lds_rd128(ub + (2 * pr + z) * 32 + 2048);
                const bf16x8 kh = lds_rd128(ko), kl = lds_rd128(ko + 6144);
                const bf16x8 kh2 = lds_rd128(ko + 512), kl2 = lds_rd128(ko + 512 + 6144);
                y0 = MFMA32(ua, kh, y0); y1 = MFMA32(ua, kh2, y1); y2 = MFMA32(ub2, kh, y2);
                y0 = MFMA32(ua, kl, y0); y1 = MFMA32(ua, kl2, y1); y2 = MFMA32(ub2, kl, y2);
            }
        }
    }
#pragma unroll
    for (int r = 0; r < 16; ++r) y1[r] += y2[r];
    TST(5)
    role_sync(cnt, 12, lane);
    {
        char* xb = lds + T_UC + w * 4096;
        const f32x16 give = wi ? y0 : y1;
#pragma unroll
        for (int g = 0; g < 4; ++g) *(f32x4*)(xb + g * 1024 + lane * 16) = f32x4{give[4 * g], give[4 * g + 1], give[4 * g + 2], give[4 * g + 3]};
    }
    role_sync(cnt, 16, lane);
    {
        const char* xb = lds + T_UC + (w ^ 1) * 4096;
        const f32x16 keep = wi ? y1 : y0;
        float* yb = (float*)(lds + T_UC + 16384) + ch * (64 * 36) + (wi * 32 + r32) * 36 + 4 * hh;
#pragma unroll
        for (int g = 0; g < 4; ++g) {
            const f32x4 v = *(const f32x4*)(xb + g * 1024 + lane * 16);
            *(f32x4*)(yb + 8 * g) = f32x4{keep[4 * g] + v[0], keep[4 * g + 1] + v[1], keep[4 * g + 2] + v[2], keep[4 * g + 3] + v[3]};
        }
    }
    role_sync(cnt, 20, lane);
    TST(6)
    {
        const float ds0 = bf2f(dskip[d0]), ds1 = bf2f(dskip[d0 + 1]);
        const float* ya = (const float*)(lds + T_UC + 16384);
#pragma unroll
        for (int k = 0; k < 8; ++k) {
            const int l = t8 + 256 * k, yi = (l >> 5) * 36 + (l & 31);
            const float u0 = __uint_as_float(up[k] << 16), u1 = __uint_as_float(up[k] & 0xffff0000u);
            const float o0 = bfround(ya[yi]) + bfround(u0 * ds0);
            const float o1 = bfround(ya[64 * 36 + yi]) + bfround(u1 * ds1);
            *(unsigned*)(out + (size_t)l * 1024 + 512 + d0) = cvtpk(o0, o1);
        }
    }
    DG1(asm volatile("s_waitcnt vmcnt(0)" ::: "memory");)
    TST(0)
    DG1({ unsigned long long tt = rstamp() - R0; if (lane == 0) { dbg[1] = tt; dbg[7] = R0; } })
}

extern "C" void kernel_launch(void* const* d_in, const int* in_sizes, int n_in, void* d_out, int out_size, void* d_ws,
                              size_t ws_size, hipStream_t stream) {
    (void)in_sizes; (void)n_in; (void)out_size; (void)ws_size;
    const unsigned short* const* in = (const unsigned short* const*)d_in;
    char* ws = (char*)d_ws;
    unsigned short* out = (unsigned short*)d_out;
    (void)hipMemsetAsync(ws + WS_FLAG, 0, 8192, stream);
    attn_kernel<<<256, 512, 0, stream>>>(in[0], in[1], in[2], in[3], in[4], in[5], ws);
    tail_kernel<<<256, 512, 0, stream>>>(ws, out, in[6], in[7], in[8], in[9], in[10], in[11], in[12], in[13], in[14]);
}
```

```cpp
#include <hip/hip_runtime.h>
#include <hip/hip_bf16.h>
#include <stdint.h>

typedef __bf16 bf16x8 __attribute__((ext_vector_type(8)));
typedef __bf16 bf16x2 __attribute__((ext_vector_type(2)));
typedef short s16x4 __attribute__((ext_vector_type(4)));
typedef float f32x16 __attribute__((ext_vector_type(16)));
typedef float f32x4 __attribute__((ext_vector_type(4)));
typedef float f32x2 __attribute__((ext_vector_type(2)));
typedef unsigned u32x4 __attribute__((ext_vector_type(4)));
typedef unsigned long long u64;
typedef const unsigned short* __restrict__ cbf;
typedef __attribute__((address_space(1))) unsigned gu32;
typedef __attribute__((address_space(1))) u64 gu64;
#define LAS __attribute__((address_space(3)))
#define DI __device__ __forceinline__
#define MFMA32(a, b, c) __builtin_amdgcn_mfma_f32_32x32x16_bf16((a), (b), (c), 0, 0, 0)
#define RLX_AGENT __ATOMIC_RELAXED, __HIP_MEMORY_SCOPE_AGENT
#define SBAR() __builtin_amdgcn_sched_barrier(0)
#define PIN(x) asm volatile("" : "+v"(x))
#define MX3(a, b, c) __builtin_fmaxf(__builtin_fmaxf((a), (b)), (c))

constexpr int DM = 512;
constexpr float QSCALE = 0.08838834764831845f * 1.4426950408889634f;
#define THRV 40.0f
constexpr float THR = THRV;
constexpr unsigned SPIN_CAP = 1u << 22;

constexpr size_t WS_Q = 0;
constexpr size_t WS_KD = 8u << 20;
constexpr size_t WS_KV = 16u << 20;
constexpr size_t WS_CTX = 48u << 20;
constexpr size_t WS_WO = 64u << 20;
constexpr size_t WS_FLAG = 72u << 20;
constexpr int C_KV = 0, C_CTX = 128, C_WO = 144, C_TICK = 145, C_NUM = 153;

DI unsigned cvtpk(float lo, float hi) { f32x2 v = {lo, hi}; bf16x2 b = __builtin_convertvector(v, bf16x2); return __builtin_bit_cast(unsigned, b); }
DI float bf2f(unsigned short b) { return __uint_as_float(((unsigned)b) << 16); }
DI float bfround(float x) { __bf16 b = (__bf16)x; return (float)b; }
DI unsigned rfl(unsigned x) { return __builtin_amdgcn_readfirstlane(x); }
DI bf16x8 pack8(const f32x16& x, int s, float sc) {
    u32x4 w = {cvtpk(x[8 * s + 0] * sc, x[8 * s + 1] * sc), cvtpk(x[8 * s + 2] * sc, x[8 * s + 3] * sc),
               cvtpk(x[8 * s + 4] * sc, x[8 * s + 5] * sc), cvtpk(x[8 * s + 6] * sc, x[8 * s + 7] * sc)};
    return __builtin_bit_cast(bf16x8, w);
}
DI bf16x8 pack8n(const f32x16& x, int s) {
    u32x4 w = {cvtpk(x[8 * s + 0], x[8 * s + 1]), cvtpk(x[8 * s + 2], x[8 * s + 3]), cvtpk(x[8 * s + 4], x[8 * s + 5]), cvtpk(x[8 * s + 6], x[8 * s + 7])};
    return __builtin_bit_cast(bf16x8, w);
}
DI float swap_sum(float x) { auto r = __builtin_amdgcn_permlane32_swap(__float_as_uint(x), __float_as_uint(x), false, false); return __uint_as_float(r[0]) + __uint_as_float(r[1]); }
DI bf16x8 lds_rd128(unsigned addr) { return *(const LAS bf16x8*)(uintptr_t)addr; }
DI s16x4 lds_tr(unsigned addr) { return __builtin_amdgcn_ds_read_tr16_b64_v4i16((LAS s16x4*)(uintptr_t)addr); }
DI bf16x8 cat8(s16x4 lo, s16x4 hi) { return __builtin_bit_cast(bf16x8, __builtin_shufflevector(lo, hi, 0, 1, 2, 3, 4, 5, 6, 7)); }

DI void glds1(const void* sbase, unsigned voff, unsigned m0v) {
    unsigned keep;
    asm volatile("s_mov_b32 %0, m0\n\ts_mov_b32 m0, %3\n\ts_nop 0\n\tglobal_load_lds_dwordx4 %1, %2\n\ts_mov_b32 m0, %0"
                 : "=&s"(keep) : "v"(voff), "s"(sbase), "s"(m0v) : "memory");
}
template <int OFF> DI void glds1m(const void* sbase, unsigned voff, unsigned m0v, int left) {
    unsigned keep;
    asm volatile("s_mov_b32 %0, m0\n\ts_mov_b32 m0, %3\n\ts_cmp_gt_i32 %4, 0\n\ts_cselect_b64 exec, -1, 0\n\t"
                 "global_load_lds_dwordx4 %1, %2 offset:%5\n\ts_mov_b64 exec, -1\n\ts_mov_b32 m0, %0"
                 : "=&s"(keep) : "v"(voff), "s"(sbase), "s"(m0v), "s"(left), "i"(OFF) : "memory", "scc");
}
DI void glds4(const void* sbase, unsigned voff, unsigned m0v) {
    unsigned keep;
    asm volatile("s_mov_b32 %0, m0\n\ts_mov_b32 m0, %3\n\ts_nop 0\n\t"
                 "global_load_lds_dwordx4 %1, %2\n\tglobal_load_lds_dwordx4 %1, %2 offset:1024\n\t"
                 "global_load_lds_dwordx4 %1, %2 offset:2048\n\tglobal_load_lds_dwordx4 %1, %2 offset:3072\n\ts_mov_b32 m0, %0"
                 : "=&s"(keep) : "v"(voff), "s"(sbase), "s"(m0v) : "memory");
}
DI void st16(char* ubase, unsigned voff, bf16x8 v) {
    __amdgpu_buffer_rsrc_t r = __builtin_amdgcn_make_buffer_rsrc(ubase, (short)0, 0x7fffffff, 0x00020000);
    __builtin_amdgcn_raw_buffer_store_b128(__builtin_bit_cast(u32x4, v), r, voff, 0, 16);
}
DI f32x16 mfma_c(bf16x8 a, bf16x8 b, const f32x16& cc) {
    f32x16 d;
    asm volatile("v_mfma_f32_32x32x16_bf16 %0, %1, %2, %3" : "=&v"(d) : "v"(a), "v"(b), "v"(cc));
    return d;
}
#define SYNC_N(n) asm volatile("s_waitcnt vmcnt(" #n ") lgkmcnt(0)\n\ts_barrier" ::: "memory")
#define SYNC_ALL() SYNC_N(0)
#define SYNC_LDS() asm volatile("s_waitcnt lgkmcnt(0)\n\ts_barrier" ::: "memory")
DI void wait_ge(gu32* p, unsigned want) {
    unsigned spins = 0;
    while (rfl(__hip_atomic_load(p, RLX_AGENT)) < want && ++spins < SPIN_CAP) __builtin_amdgcn_s_sleep(2);
}
DI u64 ack_of(u64 e, int wg) { return (e ^ 0x9E3779B97F4A7C15ull) + (u64)(wg + 1) * 0x100000001B3ull; }

#define ST(i)
#define ST_END()

struct PJ {
    const char* xs; const char* w1; const char* w2;
    const char* wp; unsigned winc, dummy;
    unsigned lb, xoff0, xoff1, woff, xdst, wdst;
    unsigned xa[4], wa[2];
};
DI void pj_x(const PJ& c, int q, unsigned xb) { glds1(c.xs + q * 8192, (q & 1) ? c.xoff1 : c.xoff0, c.xdst + xb + q * 1024); }
DI void pj_w(const PJ& c, int q, unsigned wb) { glds1((q < 2 ? c.w1 : c.w2) + (q & 1) * 32768, c.woff, c.wdst + wb + (q >> 1) * 16384 + (q & 1) * 1024); }
DI void pj_warm(PJ& c) {
    asm volatile("s_mov_b64 exec, 0xffffff\n\tglobal_load_dword %0, %1, off\n\ts_mov_b64 exec, -1" : "+v"(c.dummy) : "v"(c.wp) : "memory");
    c.wp += c.winc;
}
struct FKV { bf16x8 x[4]; s16x4 k[2], v[2]; };
struct FQ { bf16x8 x[2]; s16x4 a[2], b[2]; };
DI void kv_read(const PJ& c, FKV& f, int j, int s, unsigned xb, unsigned wb) {
    if (j == 0 || j == 1) f.k[j] = lds_tr(c.wa[0] + wb + s * 4096 + j * 1024);
    else if (j == 3 || j == 4) f.v[j - 3] = lds_tr(c.wa[0] + wb + 16384 + s * 4096 + (j - 3) * 1024);
    else { const int tb = j == 2 ? 0 : j - 4; f.x[tb] = lds_rd128(c.xa[s] + xb + tb * 4096); }
}
DI void q_read(const PJ& c, FQ& f, int j, int s, unsigned xb, unsigned wb) {
    if (j == 0 || j == 1) f.a[j] = lds_tr(c.wa[0] + wb + s * 4096 + j * 1024);
    else if (j == 3 || j == 4) f.b[j - 3] = lds_tr(c.wa[1] + wb + s * 4096 + (j - 3) * 1024);
    else { const int tb = j == 2 ? 0 : 1; f.x[tb] = lds_rd128(c.xa[s] + xb + tb * 4096); }
}

constexpr int NPRE = 6;
struct SB { f32x16 a, b; };
struct AT {
    char* ws; const char* ldp; const char* qh;
    gu32* sigp;
    unsigned kvoff; int kvneed; unsigned kvc;
    int h, bi, bj, grp, rg, qrel, w;
    unsigned lb, l16;
    unsigned ldoff, ldlim, ldbase;
    unsigned koff, voff;
    int taun, ldleft;
    bf16x8 qf[8], fr[8];
    unsigned pw[16];
    float pe[32];
    f32x16 o[4], negm;
    float mref, lsum;
};
DI void at_piece(AT& c, int q) {
    const unsigned m0v = c.ldbase + c.ldoff;
    if (q == 0) glds1m<0>(c.ldp, c.l16, m0v, c.ldleft); else if (q == 1) glds1m<1024>(c.ldp, c.l16, m0v, c.ldleft);
    else if (q == 2) glds1m<2048>(c.ldp, c.l16, m0v, c.ldleft); else glds1m<3072>(c.ldp, c.l16, m0v, c.ldleft);
}
DI void at_adv(AT& c) { c.ldp += 32768; c.ldoff += 16384; if (c.ldoff >= c.ldlim) c.ldoff = 0; c.ldleft -= 1; }
DI void at_load_tile(AT& c) { glds4(c.ldp, c.l16, c.ldbase + c.ldoff); at_adv(c); }
DI unsigned kitem(int j) { return ((j & 1) * 8 + (j >> 1)) * 1024; }
DI unsigned vitem(int j) { return ((j & 3) * 4 + (j >> 2)) * 1024; }

DI void at_mask(AT& c, SB& s, int tau, int blk) {
    const int row0 = blk * 128 + c.rg * 32;
    if (tau * 64 + 63 > row0) {
        const int dq = row0 + c.qrel - tau * 64;
#pragma unroll
        for (int r = 0; r < 16; ++r) {
            const int cc = (r & 3) + 8 * (r >> 2);
            if (cc > dq) s.a[r] = -__builtin_inff();
            if (cc + 32 > dq) s.b[r] = -__builtin_inff();
        }
    }
}
DI void at_check(AT& c, SB& s, float ma, bool first) {
    auto pr = __builtin_amdgcn_permlane32_swap(__float_as_uint(ma), __float_as_uint(ma), false, false);
    const float mx = MX3(__uint_as_float(pr[0]), __uint_as_float(pr[1]), ma);
    if (__builtin_expect(first || __any(mx > THR), 0)) {
        const float dl = first ? mx : __builtin_fmaxf(mx, 0.f);
        const float fs = first ? 1.f : __builtin_amdgcn_exp2f(-dl);
        c.mref += dl;
        c.lsum *= fs;
#pragma unroll
        for (int r = 0; r < 16; ++r) { s.a[r] -= dl; s.b[r] -= dl; c.negm[r] = -c.mref; }
        PIN(c.negm);
#pragma unroll
        for (int d = 0; d < 4; ++d)
#pragma unroll
            for (int r = 0; r < 16; ++r) c.o[d][r] *= fs;
        asm volatile("s_nop 1");
    }
}
template <bool QK> DI void at_x(AT& c, SB& sc, SB& sn) {
    const unsigned ks_ = c.lb + c.koff + c.l16, vs_ = c.lb + 65536 + c.voff + c.l16;
#pragma unroll
    for (int i = 0; i < 16; ++i) {
        const int n = i + NPRE;
        if (n < 16) { if (QK) c.fr[n & 7] = lds_rd128(ks_ + kitem(n)); }
        else c.fr[n & 7] = lds_rd128(vs_ + vitem(n - 16));
        if (QK) {
            const int s = i >> 1;
            if ((i & 1) == 0) { sn.a = s ? MFMA32(c.fr[i & 7], c.qf[s], sn.a) : mfma_c(c.fr[i & 7], c.qf[s], c.negm); PIN(sn.a); }
            else { sn.b = s ? MFMA32(c.fr[i & 7], c.qf[s], sn.b) : mfma_c(c.fr[i & 7], c.qf[s], c.negm); PIN(sn.b); }
        }
        c.pe[2 * i] = __builtin_amdgcn_exp2f(i < 8 ? sc.a[2 * i] : sc.b[2 * i - 16]);
        c.pe[2 * i + 1] = __builtin_amdgcn_exp2f(i < 8 ? sc.a[2 * i + 1] : sc.b[2 * i - 15]);
        if (i > 0) { c.pw[i - 1] = cvtpk(c.pe[2 * i - 2], c.pe[2 * i - 1]); PIN(c.pw[i - 1]); }
        PIN(c.pe[2 * i]); PIN(c.pe[2 * i + 1]);
        SBAR();
    }
    c.pw[15] = cvtpk(c.pe[30], c.pe[31]); PIN(c.pw[15]);
}
template <bool POLL> DI void at_bar(AT& c) {
    if (POLL) asm volatile("s_waitcnt vmcnt(0)\n\ts_barrier" ::: "memory");
    else asm volatile("s_waitcnt vmcnt(4)\n\ts_barrier" ::: "memory");
    if (c.sigp) {
        if (c.w == 0 && c.l16 == 0) __hip_atomic_fetch_add(c.sigp, 1u, RLX_AGENT);
        c.sigp = nullptr;
    }
    PIN(c.kvc);
    if (!POLL) return;
    const bool need = c.ldleft - 2 * c.grp <= c.kvneed && c.kvneed > 3;
    if (need && c.w == 0) {
        if (rfl(c.kvc) < 8u) wait_ge((gu32*)(c.ws + WS_FLAG + 8192 + c.kvoff), 8u);
        asm volatile("buffer_inv sc1" ::: "memory");
    }
    c.kvoff += need ? 128 : 0; c.kvneed -= need ? 8 : 0;
    asm volatile("global_load_dword %0, %1, %2 sc1" : "=v"(c.kvc) : "v"(c.kvoff), "s"(c.ws + WS_FLAG + 8192) : "memory");
}
template <bool NEXT, int NLD, bool QLD> DI float at_y(AT& c, SB& sn, int inc, int blkn) {
    if (NEXT) at_bar<true>(c);
    if (QLD) {
#pragma unroll
        for (int s = 0; s < 8; ++s) c.qf[s] = *(const bf16x8*)(c.qh + c.rg * 8192 + s * 1024 + c.l16);
    }
    if (NEXT) at_mask(c, sn, c.taun, blkn);
    const unsigned vs_ = c.lb + 65536 + c.voff + c.l16;
    c.koff = (c.koff + inc * 16384) & 65535;
    const unsigned ks_ = c.lb + c.koff + c.l16;
    float ps = c.lsum, ma = -__builtin_inff();
#pragma unroll
    for (int i = 0; i < 16; ++i) {
        const int n = i + NPRE;
        if (n < 16) c.fr[n & 7] = lds_rd128(vs_ + vitem(n));
        else if (NEXT) c.fr[n & 7] = lds_rd128(ks_ + kitem(n - 16));
        c.o[i & 3] = MFMA32(c.fr[i & 7], __builtin_bit_cast(bf16x8, u32x4{c.pw[(i >> 2) * 4], c.pw[(i >> 2) * 4 + 1], c.pw[(i >> 2) * 4 + 2], c.pw[(i >> 2) * 4 + 3]}), c.o[i & 3]);
        ps += c.pe[2 * i]; ps += c.pe[2 * i + 1];
        PIN(ps);
        if (NEXT) {
            if (i < 8) ma = MX3(ma, sn.a[2 * i], sn.a[2 * i + 1]); else ma = MX3(ma, sn.b[2 * i - 16], sn.b[2 * i - 15]);
            PIN(ma);
        }
        if ((i & 1) == 0) {
            const int q = i >> 1;
            if (NLD > (q >> 2)) { at_piece(c, q & 3); if ((q & 3) == 3) at_adv(c); }
        }
        SBAR();
    }
    c.lsum = ps;
    return ma;
}
template <int KIND, int NLD> DI void at_step(AT& c, SB& sc, SB& sn, int incv, int inck, int blkn) {
    at_x<true>(c, sc, sn);
    const float ma = at_y<true, NLD, KIND == 1>(c, sn, inck, blkn);
    c.voff += incv * 16384; if (c.voff >= 98304) c.voff -= 98304;
    c.taun += inck;
    if constexpr (KIND == 2) {
        if (!c.grp) {
            const float inv = 1.0f / swap_sum(c.lsum);
            char* dst = c.ws + WS_CTX + ((size_t)((c.bi * 4 + c.rg) * 256 + c.h * 8) << 10);
#pragma unroll
            for (int db = 0; db < 4; ++db)
#pragma unroll
                for (int sp = 0; sp < 2; ++sp) st16(dst + (db * 2 + sp) * 1024, c.l16, pack8(c.o[db], sp, inv));
#pragma unroll
            for (int k = 0; k < 4; ++k) c.o[k] = f32x16{};
            c.mref = 0.f; c.lsum = 0.f;
        }
        c.sigp = (gu32*)(c.ws + WS_FLAG + 8192) + (C_CTX + c.bi) * 32;
        at_check(c, sn, ma, !c.grp);
    } else {
        at_check(c, sn, ma, false);
    }
    if constexpr (KIND == 1) {
#pragma unroll
        for (int s = 0; s < 8; ++s) PIN(c.qf[s]);
        if (!c.grp) { c.negm = f32x16{}; PIN(c.negm); asm volatile("s_nop 1"); }
    }
}

struct cf { float r, i; };
DI cf cmul(cf a, cf b) { return cf{a.r * b.r - a.i * b.i, a.r * b.i + a.i * b.r}; }
DI cf cexp_scaled(float a2, float brev, float v) {
    const float e = __builtin_amdgcn_exp2f(a2 * v);
    const float ph = __builtin_amdgcn_fractf(brev * v);
    return cf{e * __builtin_amdgcn_cosf(ph), e * __builtin_amdgcn_sinf(ph)};
}
constexpr int GROW = 528;
constexpr int UCOPY = 4160;
constexpr int T_U2 = 64;
constexpr int T_RED = 8448;
constexpr int T_UC = T_RED + 49152;
constexpr int T_KP = T_UC + 2 * 8 * UCOPY;
constexpr int T_KD = T_KP + 2 * 12288;
constexpr int KDB = 2592;
constexpr int T_CNT = T_KD + 4 * KDB;
static_assert(T_CNT + 16 <= 163840 && T_CNT >= T_RED + 8 * 9216, "lds");
DI void role_sync(LAS unsigned* cnt, unsigned target, int lane) {
    asm volatile("s_waitcnt lgkmcnt(0)" ::: "memory");
    if (lane == 0) __hip_atomic_fetch_add(cnt, 1u, __ATOMIC_RELAXED, __HIP_MEMORY_SCOPE_WORKGROUP);
    while (__hip_atomic_load(cnt, __ATOMIC_RELAXED, __HIP_MEMORY_SCOPE_WORKGROUP) < target) __builtin_amdgcn_s_sleep(1);
    asm volatile("" ::: "memory");
}
DI void hilo_store(char* kp, int c8, const float (&kv)[8]) {
    unsigned hi[4], lo[4];
#pragma unroll
    for (int e = 0; e < 4; ++e) {
        hi[e] = cvtpk(kv[2 * e], kv[2 * e + 1]);
        lo[e] = cvtpk(kv[2 * e] - __uint_as_float(hi[e] << 16), kv[2 * e + 1] - __uint_as_float(hi[e] & 0xffff0000u));
    }
    const int off = (c8 & 3) * 1536 + ((c8 >> 2) + 32) * 16;
    *(u32x4*)(kp + off) = u32x4{hi[0], hi[1], hi[2], hi[3]};
    *(u32x4*)(kp + 6144 + off) = u32x4{lo[0], lo[1], lo[2], lo[3]};
}
constexpr int OD = 8;

#define DIAGARGS
#define DIAGPARAMS
DI void tail_part(AT& c, SB& sdummy, char* lds, unsigned ticket DIAGPARAMS) {
    unsigned lane_;
    asm volatile("v_mbcnt_lo_u32_b32 %0, -1, 0\n\tv_mbcnt_hi_u32_b32 %0, -1, %0" : "=v"(lane_));
    typedef const void* const __attribute__((address_space(4)))* kargs_t;
    kargs_t ka = (kargs_t)__builtin_amdgcn_kernarg_segment_ptr();
    asm volatile("" : "+s"(ka));
    cbf are = (cbf)ka[7], aim = (cbf)ka[8], bre = (cbf)ka[9], bim = (cbf)ka[10], cre = (cbf)ka[11], cim = (cbf)ka[12],
        logdt = (cbf)ka[13], dskip = (cbf)ka[14];
    unsigned short* __restrict__ out = (unsigned short*)ka[16];
    char* ws = c.ws;
    const int lane = lane_, w = c.w, tid = w * 64 + lane, r32 = lane & 31, hh = lane >> 5, grp = c.grp, rg = c.rg, h = c.h, bj = c.bj;
    const unsigned l16 = lane * 16;
    const int b = blockIdx.x, xcd = b & 7, idx = b >> 3;
    gu32* ctr = (gu32*)(ws + WS_FLAG + 8192);
    const int d0 = xcd * 64 + idx * 2;
    unsigned prm[2][6], pdt = 0, dsk = 0, rdy;
    ticket = rfl(ticket);
    const int mb0 = (ticket < 16 ? 0 : 16) + xcd * 2 + ((ticket >> 3) & 1);
    asm volatile("global_load_dword %0, %1, %2 sc1" : "=v"(rdy) : "v"((unsigned)((lane & 1) ? C_CTX + (mb0 >> 1) : C_WO) * 128u), "s"(ws + WS_FLAG + 8192) : "memory");
    if (grp) {
        const int d = d0 + (rg >> 1);
        pdt = logdt[d]; dsk = *(const unsigned*)(dskip + d0);
#pragma unroll
        for (int z = 0; z < 2; ++z) {
            const int o = d * 128 + (rg & 1) * 64 + z * 32 + r32;
            prm[z][0] = are[o]; prm[z][1] = aim[o]; prm[z][2] = bre[o]; prm[z][3] = bim[o]; prm[z][4] = cre[o]; prm[z][5] = cim[o];
        }
    }
    SBAR();
    at_y<false, 0, false>(c, sdummy, 0, bj);
    ST(7)
    LAS unsigned* cnts = (LAS unsigned*)(uintptr_t)(c.lb + T_CNT);
    {
        const float lsum = swap_sum(c.lsum), mref = c.mref;
        SYNC_LDS();
        char* mine = lds + T_RED + w * 9216;
#pragma unroll
        for (int d = 0; d < 2; ++d) {
            const f32x16 oa = c.o[d], ob = c.o[2 + d];
            const f32x16 src = grp ? oa : ob;
#pragma unroll
            for (int g = 0; g < 4; ++g)
                *(f32x4*)(mine + (d * 4 + g) * 1024 + l16) = f32x4{src[4 * g], src[4 * g + 1], src[4 * g + 2], src[4 * g + 3]};
        }
        *(f32x2*)(mine + 8192 + lane * 8) = f32x2{mref, lsum};
        asm volatile("s_waitcnt vmcnt(0)" : "+v"(rdy) :: "memory");
        if (w == 0) {
            const bool ok = __all(rdy >= ((lane & 1) ? 32u : 256u));
            if (lane == 0) { cnts[0] = 0; cnts[1] = 0; cnts[2] = ticket | (ok ? 256u : 0u); cnts[3] = 0; }
            if (lane < 16) *(unsigned*)(lds + lane * 4) = 0u;
        }
        SYNC_LDS();
        const char* oth = lds + T_RED + (w ^ 4) * 9216;
        const f32x2 ml = *(const f32x2*)(oth + 8192 + lane * 8);
        const float m = fmaxf(mref, ml[0]);
        const float fa = __builtin_amdgcn_exp2f(mref - m), fb = __builtin_amdgcn_exp2f(ml[0] - m);
        const float inv = 1.0f / (lsum * fa + ml[1] * fb);
        const float ca = fa * inv, cb = fb * inv;
        char* dst = ws + WS_CTX + ((size_t)((bj * 4 + rg) * 256 + h * 8 + (grp ? 4 : 0)) << 10);
#pragma unroll
        for (int d = 0; d < 2; ++d) {
            const f32x16 oa = c.o[d], ob = c.o[2 + d];
            f32x16 acc = grp ? ob : oa;
#pragma unroll
            for (int g = 0; g < 4; ++g) {
                const f32x4 v = *(const f32x4*)(oth + (d * 4 + g) * 1024 + l16);
#pragma unroll
                for (int e = 0; e < 4; ++e) acc[4 * g + e] = acc[4 * g + e] * ca + v[e] * cb;
            }
#pragma unroll
            for (int sp = 0; sp < 2; ++sp) st16(dst + (d * 2 + sp) * 1024, l16, pack8n(acc, sp));
        }
    }
    const unsigned tk = rfl(cnts[2]);
    SYNC_LDS();
    ST(8)
#define CTX_SIGNAL() { asm volatile("s_waitcnt vmcnt(0)" ::: "memory");                                                          \
        if (lane == 0 && __hip_atomic_fetch_add(cnts + 3, 1u, __ATOMIC_RELAXED, __HIP_MEMORY_SCOPE_WORKGROUP) == 7u)             \
            __hip_atomic_fetch_add(ctr + (C_CTX + bj) * 32, 1u, RLX_AGENT); }
    if (!grp) {
        const int mb = ((tk & 255) < 16 ? 0 : 16) + xcd * 2 + ((tk >> 3) & 1), nb = tk & 7;
        const bool early = tk >= 256;
        if (!early) {
            CTX_SIGNAL()
            if (w == 0) {
                gu32* pp = ctr + (lane & 1 ? C_CTX + (mb >> 1) : C_WO) * 32;
                const unsigned want = lane & 1 ? 32u : 256u;
                unsigned spins = 0;
                while (!__all(__hip_atomic_load(pp, RLX_AGENT) >= want) && ++spins < SPIN_CAP) __builtin_amdgcn_s_sleep(2);
            }
            role_sync(cnts, 4, lane);
        }
        __builtin_amdgcn_fence(__ATOMIC_ACQUIRE, "wavefront");
        ST(9)
        const __amdgpu_buffer_rsrc_t ra = __builtin_amdgcn_make_buffer_rsrc(ws + WS_CTX + ((size_t)((mb * 2) * 256 + w * 64) << 10), (short)0, 0x7fffffff, 0x00020000);
        const __amdgpu_buffer_rsrc_t rb_ = __builtin_amdgcn_make_buffer_rsrc(ws + WS_WO + ((size_t)((nb * 2) * 256 + w * 64) << 10), (short)0, 0x7fffffff, 0x00020000);
#define LDA(off) __builtin_bit_cast(bf16x8, __builtin_amdgcn_raw_buffer_load_b128(ra, l16, (off), 16))
#define LDB(off) __builtin_bit_cast(bf16x8, __builtin_amdgcn_raw_buffer_load_b128(rb_, l16, (off), 16))
        f32x16 acc[4];
#pragma unroll
        for (int i = 0; i < 4; ++i) acc[i] = f32x16{};
        bf16x8 rgn[OD][4];
#pragma unroll
        for (int k = 0; k < OD; ++k) {
            rgn[k][0] = LDA(k * 1024); rgn[k][1] = LDB(k * 1024);
            rgn[k][2] = LDA(262144 + k * 1024); rgn[k][3] = LDB(262144 + k * 1024);
        }
        SBAR();
        if (early) {
            asm volatile("s_waitcnt vmcnt(32)" ::: "memory");
            if (lane == 0 && __hip_atomic_fetch_add(cnts + 3, 1u, __ATOMIC_RELAXED, __HIP_MEMORY_SCOPE_WORKGROUP) == 7u)
                __hip_atomic_fetch_add(ctr + (C_CTX + bj) * 32, 1u, RLX_AGENT);
        }
        SBAR();
#pragma unroll 1
        for (int k0 = 0; k0 < 64 - OD; k0 += OD) {
#pragma unroll
            for (int k = 0; k < OD; ++k) {
                const bf16x8 c0 = rgn[k][0], w0 = rgn[k][1], c1 = rgn[k][2], w1 = rgn[k][3];
                acc[0] = MFMA32(w0, c0, acc[0]);
                acc[1] = MFMA32(w1, c0, acc[1]);
                acc[2] = MFMA32(w0, c1, acc[2]);
                acc[3] = MFMA32(w1, c1, acc[3]);
                SBAR();
                const int kn = (k0 + OD + k) * 1024;
                rgn[k][0] = LDA(kn); rgn[k][1] = LDB(kn);
                rgn[k][2] = LDA(262144 + kn); rgn[k][3] = LDB(262144 + kn);
                SBAR();
            }
        }
#pragma unroll
        for (int k = 0; k < OD; ++k) {
            acc[0] = MFMA32(rgn[k][1], rgn[k][0], acc[0]);
            acc[1] = MFMA32(rgn[k][3], rgn[k][0], acc[1]);
            acc[2] = MFMA32(rgn[k][1], rgn[k][2], acc[2]);
            acc[3] = MFMA32(rgn[k][3], rgn[k][2], acc[3]);
        }
        ST(10)
#pragma unroll
        for (int i = 0; i < 4; ++i) {
            if (i != w) {
                const int slot = w < i ? w : w - 1;
#pragma unroll
                for (int g = 0; g < 4; ++g)
                    *(f32x4*)(lds + T_RED + (i * 3 + slot) * 4096 + g * 1024 + l16) = f32x4{acc[i][4 * g], acc[i][4 * g + 1], acc[i][4 * g + 2], acc[i][4 * g + 3]};
            }
        }
        role_sync(cnts, early ? 4 : 8, lane);
        const f32x16 own = w == 0 ? acc[0] : (w == 1 ? acc[1] : (w == 2 ? acc[2] : acc[3]));
        unsigned pk[8];
#pragma unroll
        for (int g = 0; g < 4; ++g) {
            f32x4 sm = f32x4{own[4 * g], own[4 * g + 1], own[4 * g + 2], own[4 * g + 3]};
#pragma unroll
            for (int sl = 0; sl < 3; ++sl) sm += *(const f32x4*)(lds + T_RED + (w * 3 + sl) * 4096 + g * 1024 + l16);
            pk[2 * g] = cvtpk(sm[0], sm[1]); pk[2 * g + 1] = cvtpk(sm[2], sm[3]);
        }
        const int q = mb * 64 + (w >> 1) * 32 + r32;
        unsigned short* orow = out + (size_t)q * 1024 + nb * 64 + (w & 1) * 32;
#pragma unroll
        for (int gp = 0; gp < 2; ++gp) {
            auto r0 = __builtin_amdgcn_permlane32_swap(pk[4 * gp + 0], pk[4 * gp + 2], false, false);
            auto r1 = __builtin_amdgcn_permlane32_swap(pk[4 * gp + 1], pk[4 * gp + 3], false, false);
            *(u32x4*)(orow + gp * 16 + hh * 8) = u32x4{r0[0], r1[0], r0[1], r1[1]};
        }
        ST_END()
        return;
    }
    const int t8 = tid - 256, ch = rg >> 1, wi = rg & 1, t7 = t8 & 127;
    LAS unsigned* cnt = cnts + 1;
    CTX_SIGNAL()
    {
        const float dt = bfround(expf(bf2f(pdt)));
        const int i16 = lane & 15, g4 = lane >> 4;
        const int wx = (r32 >> 1) ^ (hh * 8);
        char* scr = lds + T_UC + rg * 4096;
        char* wr0 = scr + hh * 2048 + (r32 & 1) * 8;
        f32x4 res[3][2];
#pragma unroll
        for (int T = 0; T < 3; ++T) { res[T][0] = f32x4{}; res[T][1] = f32x4{}; }
#pragma unroll 1
        for (int z = 0; z < 2; ++z) {
#define PZ(i) bf2f(z ? prm[1][i] : prm[0][i])
            const float a2 = dt * PZ(0) * 1.4426950408889634f, brev = dt * PZ(1) * 0.15915494309189535f;
            const cf coef = cmul(cf{PZ(4), PZ(5)}, cf{dt * PZ(2), dt * PZ(3)});
            const cf e1 = cexp_scaled(a2, brev, 1.f), e8 = cexp_scaled(a2, brev, 8.f), e128 = cexp_scaled(a2, brev, 128.f);
            const cf e2 = cmul(e1, e1), e4 = cmul(e2, e2);
            f32x4 fb[4];
#pragma unroll
            for (int T = 0; T < 4; ++T) {
                const float vs = T == 0 ? (hh ? 64.f : 0.f) : T == 1 ? (hh ? 128.f : 0.f) : T == 2 ? (hh ? 512.f : 256.f) : (hh ? 2048.f : 1024.f);
                cf g = cexp_scaled(a2, brev, vs);
                if (T) g = cmul(g, coef);
                const cf rb_ = T == 0 ? e8 : T == 1 ? e1 : T == 3 ? e128 : cf{hh ? e128.r : e2.r, hh ? e128.i : e2.i};
                const cf g4_ = T == 2 ? cmul(g, cf{hh ? e4.r : e128.r, hh ? e4.i : e128.i}) : cf{0.f, 0.f};
                asm volatile("" ::: "memory");
#pragma unroll
                for (int r = 0; r < 8; ++r) {
                    *(f32x2*)(wr0 + r * 256 + ((wx ^ r) << 4)) = f32x2{g.r, T ? -g.i : g.i};
                    if (r < 7) g = (T == 2 && r == 3) ? g4_ : cmul(g, rb_);
                }
                asm volatile("" ::: "memory");
                f32x4 ga[4];
#pragma unroll
                for (int kk = 0; kk < 4; ++kk) ga[kk] = *(const f32x4*)(scr + i16 * 256 + (((kk * 4 + g4) ^ i16) << 4));
                if (T == 0) {
#pragma unroll
                    for (int kk = 0; kk < 4; ++kk) fb[kk] = ga[kk];
                } else {
#pragma unroll
                    for (int kk = 0; kk < 4; ++kk) {
                        res[T - 1][0] = __builtin_amdgcn_mfma_f32_16x16x4f32(ga[kk][0], fb[kk][0], res[T - 1][0], 0, 0, 0);
                        res[T - 1][1] = __builtin_amdgcn_mfma_f32_16x16x4f32(ga[kk][1], fb[kk][1], res[T - 1][1], 0, 0, 0);
                        res[T - 1][0] = __builtin_amdgcn_mfma_f32_16x16x4f32(ga[kk][2], fb[kk][2], res[T - 1][0], 0, 0, 0);
                        res[T - 1][1] = __builtin_amdgcn_mfma_f32_16x16x4f32(ga[kk][3], fb[kk][3], res[T - 1][1], 0, 0, 0);
                    }
                }
            }
        }
        float* kd = (float*)(lds + T_KD + rg * KDB);
#pragma unroll
        for (int e = 0; e < 4; ++e) {
            const int i = g4 * 4 + e, j = i16;
            const int id1 = i < 8 ? 256 + (i >> 2) * 64 + (i & 3) + 4 * j : 384 + (i & 3) * 32 + ((i >> 2) & 1) + 2 * j;
            const int id2 = 512 + i * 16 + j;
            kd[(i >> 3) * 128 + (i & 7) + 8 * j] = res[0][0][e] + res[0][1][e];
            kd[id1] = res[1][0][e] + res[1][1][e];
            if (id2 <= 640) kd[id2] = res[2][0][e] + res[2][1][e];
        }
    }
    ST(9)
    role_sync(cnt, 4, lane);
    ST(10)
    char* kp = lds + T_KP + ch * 12288;
    char* uc = lds + T_UC + ch * (8 * UCOPY);
    {
        const float* kd0 = (const float*)(lds + T_KD + (ch * 2) * KDB), * kd1 = (const float*)(lds + T_KD + (ch * 2 + 1) * KDB);
        float kv[8];
        if (wi == 0) {
            const int base = lane < 32 ? 8 * lane : 128 + 4 * lane;
            const f32x4 a0 = *(const f32x4*)(kd0 + base) + *(const f32x4*)(kd1 + base);
            const f32x4 a1 = *(const f32x4*)(kd0 + base + 4) + *(const f32x4*)(kd1 + base + 4);
            const bool ex = lane < 32;
            kv[0] = a0[0]; kv[1] = ex ? a0[1] : a0[0]; kv[2] = ex ? a0[2] : a0[1]; kv[3] = ex ? a0[3] : a0[2];
            kv[4] = ex ? a1[0] : a0[2]; kv[5] = ex ? a1[1] : a0[2]; kv[6] = ex ? a1[2] : a0[3]; kv[7] = ex ? a1[3] : a1[0];
        } else {
            const int base = 256 + 2 * t7;
            const f32x2 a0 = *(const f32x2*)(kd0 + base) + *(const f32x2*)(kd1 + base);
            const float a2 = kd0[base + 2] + kd1[base + 2];
            kv[0] = kv[1] = kv[2] = a0[0]; kv[3] = kv[4] = kv[5] = a0[1]; kv[6] = kv[7] = a2;
        }
        hilo_store(kp, t7, kv);
        {
            const int c8 = 128 + t7, base = 384 + c8;
            const float a0 = kd0[base] + kd1[base], a1 = kd0[base + 1] + kd1[base + 1];
            kv[0] = kv[1] = kv[2] = kv[3] = a0; kv[4] = (c8 & 1) ? a1 : a0; kv[5] = kv[6] = kv[7] = a1;
            hilo_store(kp, c8, kv);
        }
        {
            const int pl = t7 >> 5, rr = t7 & 31;
            *(u32x4*)(kp + pl * 1536 + rr * 16) = u32x4{0, 0, 0, 0};
            *(u32x4*)(kp + 6144 + pl * 1536 + rr * 16) = u32x4{0, 0, 0, 0};
        }
        const unsigned sel = ch ? 0x03020706u : 0x01000504u;
#pragma unroll 1
        for (int it = 0; it < 2; ++it) {
            const int X = t7 + it * 128;
            const char* ul = lds + T_U2 + X * 32 - 32;
            const u32x4 x0 = *(const u32x4*)(ul), x1 = *(const u32x4*)(ul + 16), x2 = *(const u32x4*)(ul + 32), x3 = *(const u32x4*)(ul + 48);
            const unsigned xx[16] = {x0[0], x0[1], x0[2], x0[3], x1[0], x1[1], x1[2], x1[3], x2[0], x2[1], x2[2], x2[3], x3[0], x3[1], x3[2], x3[3]};
            unsigned dd[8];
#pragma unroll
            for (int k = 0; k < 8; ++k) dd[k] = __builtin_amdgcn_perm(xx[15 - 2 * k], xx[14 - 2 * k], sel);
#pragma unroll
            for (int cc = 0; cc < 8; ++cc) {
                const int e = 7 - cc;
                u32x4 v;
#pragma unroll
                for (int k = 0; k < 4; ++k)
                    v[k] = (e & 1) ? __builtin_amdgcn_alignbit(dd[(e + 1) / 2 + k], dd[(e - 1) / 2 + k], 16) : dd[e / 2 + k];
                *(u32x4*)(uc + cc * UCOPY + (X + 3) * 16) = v;
            }
        }
        if (t7 < 24) *(u32x4*)(uc + (t7 & 7) * UCOPY + (t7 >> 3) * 16) = u32x4{0, 0, 0, 0};
    }
    role_sync(cnt, 8, lane);
    ST(11)
    f32x16 y0 = f32x16{}, y1 = f32x16{}, y2 = f32x16{};
    {
        const int tb = -1 + 32 * wi;
        unsigned ub = (unsigned)(uintptr_t)uc + (r32 & 7) * UCOPY + ((r32 >> 3) - hh + 3) * 16 + tb * 32;
        unsigned kb = (unsigned)(uintptr_t)kp + hh * 1536 + (r32 + 32 - 16 * wi - 1) * 16;
        bf16x8 fa[6], fbn[6];
#define CV_ADDR(i, j) ((j) == 0 ? ub + (i) * 32 : (j) == 3 ? ub + (i) * 32 + 2048 :                                   \
                       kb + ((i) < 2 ? 16 : 0) + (((i) & 1) ? 0 : 3072) + ((j) == 2 || (j) == 5 ? 512 : 0) + ((j) >= 4 ? 6144 : 0))
#define CV_STEP(fc, fn, i)                                                                                              \
        _Pragma("unroll") for (int j = 0; j < 6; ++j) {                                                                 \
            fn[j] = lds_rd128(CV_ADDR((i) + 1, j));                                                                     \
            if (j == 0) { y0 = MFMA32(fc[0], fc[1], y0); PIN(y0); } else if (j == 1) { y1 = MFMA32(fc[0], fc[2], y1); PIN(y1); }   \
            else if (j == 2) { y2 = MFMA32(fc[3], fc[1], y2); PIN(y2); } else if (j == 3) { y0 = MFMA32(fc[0], fc[4], y0); PIN(y0); } \
            else if (j == 4) { y1 = MFMA32(fc[0], fc[5], y1); PIN(y1); } else { y2 = MFMA32(fc[3], fc[4], y2); PIN(y2); } \
            SBAR();                                                                                                     \
        }
#pragma unroll
        for (int j = 0; j < 6; ++j) fa[j] = lds_rd128(CV_ADDR(0, j));
#pragma unroll 1
        for (int pr = 0; pr < 16; ++pr) {
            CV_STEP(fa, fbn, 0) CV_STEP(fbn, fa, 1)
            ub += 64; kb -= 16;
        }
    }
#pragma unroll
    for (int r = 0; r < 16; ++r) y1[r] += y2[r];
    ST(12)
    role_sync(cnt, 12, lane);
    {
        char* xb = lds + T_UC + rg * 4096;
        const f32x16 give = wi ? y0 : y1;
#pragma unroll
        for (int g = 0; g < 4; ++g) *(f32x4*)(xb + g * 1024 + l16) = f32x4{give[4 * g], give[4 * g + 1], give[4 * g + 2], give[4 * g + 3]};
    }
    role_sync(cnt, 16, lane);
    {
        const char* xb = lds + T_UC + (rg ^ 1) * 4096;
        const f32x16 keep = wi ? y1 : y0;
        float* yb = (float*)(lds + T_UC + 16384) + ch * (64 * 36) + (wi * 32 + r32) * 36 + 4 * hh;
#pragma unroll
        for (int g = 0; g < 4; ++g) {
            const f32x4 v = *(const f32x4*)(xb + g * 1024 + l16);
            *(f32x4*)(yb + 8 * g) = f32x4{keep[4 * g] + v[0], keep[4 * g + 1] + v[1], keep[4 * g + 2] + v[2], keep[4 * g + 3] + v[3]};
        }
    }
    role_sync(cnt, 20, lane);
    ST(13)
    {
        const float ds0 = __uint_as_float(dsk << 16), ds1 = __uint_as_float(dsk & 0xffff0000u);
        const float* ya = (const float*)(lds + T_UC + 16384);
#pragma unroll 2
        for (int k = 0; k < 8; ++k) {
            const int l = t8 + 256 * k, yi = (l >> 5) * 36 + (l & 31);
            const unsigned upk = *(const unsigned*)(lds + T_U2 + l * 4);
            const float u0 = __uint_as_float(upk << 16), u1 = __uint_as_float(upk & 0xffff0000u);
            const float o0 = bfround(ya[yi]) + bfround(u0 * ds0);
            const float o1 = bfround(ya[64 * 36 + yi]) + bfround(u1 * ds1);
            *(unsigned*)(out + (size_t)l * 1024 + 512 + d0) = cvtpk(o0, o1);
        }
    }
    ST_END()
}

__global__ __launch_bounds__(512, 2) void mega_kernel(cbf x, cbf ckv, cbf wq, cbf wuk, cbf wuv, cbf wo, cbf u, cbf are, cbf aim, cbf bre,
                                                      cbf bim, cbf cre, cbf cim, cbf logdt, cbf dskip, char* __restrict__ ws,
                                                      unsigned short* __restrict__ out) {
    __shared__ __attribute__((aligned(16))) char lds[163840];
    const int tid = threadIdx.x, lane = tid & 63, w = rfl(tid >> 6), r32 = lane & 31, hh = lane >> 5;
    const int b = blockIdx.x, xcd = b & 7, idx = b >> 3, h = xcd * 4 + (idx & 3), rb = idx >> 2;
    const int bi = rb, bj = 15 - rb, grp = w >> 2, rg = w & 3;
    const unsigned l16 = lane * 16;
    gu64* epoch = (gu64*)(ws + WS_FLAG);
    gu64* acks = (gu64*)(ws + WS_FLAG + 4096);
    gu32* ctr = (gu32*)(ws + WS_FLAG + 8192);
    AT c;
    c.ws = ws; c.h = h; c.bi = bi; c.bj = bj; c.grp = grp; c.rg = rg; c.qrel = r32 - 4 * hh; c.w = w; c.sigp = nullptr;
    c.lb = (unsigned)(uintptr_t)lds; c.l16 = l16;
    c.ldp = ws + WS_KV + ((size_t)h << 20) + w * 4096;
    c.qh = ws + WS_Q + ((size_t)b << 15);
    c.ldbase = c.lb + grp * 65536 + rg * 4096;
    c.ldoff = grp ? 32768 : 0; c.ldlim = grp ? 98304 : 65536; c.koff = 16384; c.voff = 32768; c.taun = 1; c.ldleft = 32 - 2 * bi;
    {
    constexpr unsigned S0 = 0u, S1 = 32768u, S2 = 65536u, S3 = 98304u, S4 = 131072u;
    PJ p;
    p.lb = c.lb;
    {
        const int r8 = lane >> 3, sl8 = lane & 7, r4 = lane >> 4, sl16 = lane & 15;
        p.xoff0 = r8 * 1024 + ((sl8 ^ (r8 >> 1)) << 4);
        p.xoff1 = r8 * 1024 + ((sl8 ^ (4 + (r8 >> 1))) << 4);
        p.woff = r4 * 8192 + ((sl16 ^ (r4 << 2)) << 4);
    }
    p.xdst = p.lb + w * 4096; p.wdst = p.lb + w * 2048;
    const size_t wrow_off = (size_t)(w * 8) * 8192 + h * 256;
    p.xs = (const char*)ckv + (size_t)((rg * 8 + rb) * 64 + grp * 32) * 1024; p.w1 = (const char*)wuk + wrow_off; p.w2 = (const char*)wuv + wrow_off;
#define XADV() p.xs += 128
#define WADV() { p.w1 += 64 * 8192; p.w2 += 64 * 8192; }
    const int sh = idx * 8 + w;
    const size_t warm_x = (size_t)(sh * 8 + (lane & 7)) * 1024 + ((lane >> 3) & 1) * 128;
    const size_t warm_w = (size_t)(sh >> 1) * 8192 + xcd * 1024 + ((sh & 1) * 4 + (lane & 3)) * 128;
    p.winc = lane < 16 ? 256u : 128u * 8192u;
    p.wp = (lane < 16 ? (const char*)ckv + warm_x : (lane < 20 ? (const char*)wuk : (const char*)wuv) + warm_w) + (size_t)p.winc;
    p.dummy = 0;
    const unsigned vzero = 0;
    u64 ep;
    asm volatile("global_load_dwordx2 %0, %1, %2 sc1" : "=v"(ep) : "v"(vzero), "s"(epoch) : "memory");
    if (b == 0 && w == 0) {
#pragma unroll
        for (int k = 0; k < 3; ++k) if (k * 64 + lane < C_NUM) __hip_atomic_store(ctr + (k * 64 + lane) * 32, 0u, RLX_AGENT);
    }
#pragma unroll
    for (int t = 0; t < 2; ++t) {
#pragma unroll
        for (int q = 0; q < 4; ++q) { pj_x(p, q, t ? S1 : S0); pj_w(p, q, t ? S3 : S2); }
        XADV(); WADV();
    }
    pj_warm(p);
    asm volatile("s_waitcnt vmcnt(9)" ::: "memory");
    PIN(ep);
    ep = ((u64)rfl((unsigned)(ep >> 32)) << 32) | rfl((unsigned)ep);
    if (tid == 0) __hip_atomic_store(acks + b, ack_of(ep, b), RLX_AGENT);
    ST(14)
    const int g16 = (lane >> 4) & 1, q4 = (lane & 15) >> 2, p4 = lane & 3;
#define WBASE(dq) (p.lb + (8 * hh + q4) * 256 + ((((dq) ^ q4) * 4 + g16 * 2 + (p4 >> 1)) << 4) + (p4 & 1) * 8)
    const int th = w >> 2, dq = w & 3;
    const int tq = w >> 1, dh = w & 1;
    {
        const int f = (r32 >> 1) & 7;
#pragma unroll
        for (int s = 0; s < 4; ++s) p.xa[s] = p.lb + th * 16384 + r32 * 128 + (((2 * s + hh) ^ f) << 4);
    }
    p.wa[0] = WBASE(dq);
    SYNC_LDS();
    ST(2)
    FQ g0, g1;
    bf16x8 kvp[16];
    {
        f32x16 acck[4], accv[4];
#pragma unroll
        for (int i = 0; i < 4; ++i) { acck[i] = f32x16{}; accv[i] = f32x16{}; }
        FKV f0, f1;
#pragma unroll
        for (int j = 0; j < 8; ++j) kv_read(p, f0, j, 0, S0, S2);
#define KV_STEP(fc, fn, sn, xb, wb, RD, H1, H2, H3)                                                             \
        {                                                                                                       \
            const bf16x8 wk = cat8(fc.k[0], fc.k[1]), wv = cat8(fc.v[0], fc.v[1]);                              \
            _Pragma("unroll") for (int j = 0; j < 8; ++j) {                                                     \
                if (RD) kv_read(p, fn, j, sn, xb, wb);                                                          \
                if (j & 1) { accv[j >> 1] = MFMA32(fc.x[j >> 1], wv, accv[j >> 1]); PIN(accv[j >> 1]); }        \
                else { acck[j >> 1] = MFMA32(wk, fc.x[j >> 1], acck[j >> 1]); PIN(acck[j >> 1]); }              \
                if (j == 1) { H1; } if (j == 4) { H2; } if (j == 6) { H3; }                                     \
                SBAR();                                                                                         \
            }                                                                                                   \
        }
        KV_STEP(f0, f1, 1, S0, S2, true, pj_warm(p), , )
        KV_STEP(f1, f0, 2, S0, S2, true, pj_w(p, 0, S4), pj_w(p, 1, S4), )
        KV_STEP(f0, f1, 3, S0, S2, true, pj_w(p, 2, S4), pj_w(p, 3, S4), WADV())
        SYNC_N(5);
        KV_STEP(f1, f0, 0, S1, S3, true, pj_x(p, 0, S0), pj_x(p, 1, S0), )
        unsigned wcur = S3, wnext = S4, wld = S2;
#pragma unroll 1
        for (int t = 1; t < 6; ++t) {
            const unsigned xb = (t & 1) << 15, xn = xb ^ S1;
            if (t == 2) p.wp = lane < 16 ? (const char*)x + warm_x : (const char*)wq + warm_w;
            KV_STEP(f0, f1, 1, xb, wcur, true, pj_x(p, 2, xn), pj_x(p, 3, xn); XADV(), pj_warm(p))
            KV_STEP(f1, f0, 2, xb, wcur, true, pj_w(p, 0, wld), pj_w(p, 1, wld), )
            KV_STEP(f0, f1, 3, xb, wcur, true, pj_w(p, 2, wld), pj_w(p, 3, wld), WADV())
            SYNC_N(5);
            KV_STEP(f1, f0, 0, xn, wnext, true, pj_x(p, 0, xb), pj_x(p, 1, xb), )
            const unsigned tmp = wcur; wcur = wnext; wnext = wld; wld = tmp;
        }
        if (b == 0 && w == 0) {
            unsigned spins = 0;
            for (;;) {
                bool ok = true;
#pragma unroll
                for (int k = 0; k < 4; ++k) ok &= __hip_atomic_load(acks + k * 64 + lane, RLX_AGENT) == ack_of(ep, k * 64 + lane);
                if (__all(ok) || ++spins >= SPIN_CAP) break;
                __builtin_amdgcn_s_sleep(2);
            }
            asm volatile("s_waitcnt vmcnt(0)" ::: "memory");
            if (lane == 0) __hip_atomic_store(epoch, ep + 1, RLX_AGENT);
        }
        SBAR();
#define WO_DMA(q) glds1((const char*)wo + (size_t)(b * 16 + w * 2 + (q)) * 1024, l16, p.lb + S4 + 16384 + (w * 2 + (q)) * 1024)
        p.w1 = (const char*)wq + wrow_off;
        KV_STEP(f0, f1, 1, S0, S2, true, pj_x(p, 2, S1), pj_x(p, 3, S1), )
        p.xs = (const char*)x + (size_t)((grp ? bj : bi) * 128 + rg * 32) * 1024;
        KV_STEP(f1, f0, 2, S0, S2, true, pj_w(p, 0, S4), pj_w(p, 1, S4), WADV())
        KV_STEP(f0, f1, 3, S0, S2, true, WO_DMA(0), WO_DMA(1), )
        SYNC_N(4);
        KV_STEP(f1, f0, 0, S1, S3, true, pj_x(p, 0, S0), pj_x(p, 1, S0), )
        KV_STEP(f0, f1, 1, S1, S3, true, pj_x(p, 2, S0), pj_x(p, 3, S0), XADV())
        KV_STEP(f1, f0, 2, S1, S3, true, pj_w(p, 0, S2), pj_w(p, 1, S2), WADV())
        KV_STEP(f0, f1, 3, S1, S3, true, pj_w(p, 0, S2 + 16384), pj_w(p, 1, S2 + 16384), WADV())
        SYNC_N(4);
        asm volatile("" :: "v"(p.dummy));
        KV_STEP(f1, f0, 0, 0u, 0u, false, pj_x(p, 0, S1), pj_x(p, 1, S1), )
        ST(3)
        p.wa[0] = WBASE(dh * 2); p.wa[1] = WBASE(dh * 2 + 1);
#pragma unroll
        for (int s = 0; s < 4; ++s) p.xa[s] += tq * 8192 - th * 16384;
#pragma unroll
        for (int j = 0; j < 6; ++j) q_read(p, g0, j, 0, S0, S4);
#pragma unroll
        for (int tb = 0; tb < 4; ++tb)
#pragma unroll
            for (int sp = 0; sp < 2; ++sp) { kvp[tb * 4 + sp * 2] = pack8n(acck[tb], sp); kvp[tb * 4 + sp * 2 + 1] = pack8n(accv[tb], sp); }
    }
    ST(4)
    {
        f32x16 acc[4];
#pragma unroll
        for (int i = 0; i < 4; ++i) acc[i] = f32x16{};
        char* kvt = ws + WS_KV + ((size_t)(h * 32 + rb) << 15);
#define KVST(i) st16(kvt + ((size_t)((i) >> 2) << 18) + (((i) & 1) ? 16384 + ((dq * 4 + th * 2 + (((i) >> 1) & 1)) << 10)              \
                                                                  : ((th * 8 + dq * 2 + (((i) >> 1) & 1)) << 10)), l16, kvp[i])
#define KVSIG(g) if (tid == 0) __hip_atomic_fetch_add(ctr + (C_KV + h * 4 + (g)) * 32, 1u, RLX_AGENT);
#define WO_ST(it) { const int nb = w + (it) * 8; const unsigned a = p.lb + S4 + 16384 + (4 * hh + q4) * 1024 + nb * 64 + g16 * 32 + p4 * 8;  \
                    st16(ws + WS_WO + ((size_t)(nb * 256 + b) << 10), l16, cat8(lds_tr(a), lds_tr(a + 8192))); }
#define Q_STEP(fc, fn, sn, xb, wb, RD, H0, H1, H2, H3)                                                          \
        {                                                                                                       \
            const bf16x8 w0 = cat8(fc.a[0], fc.a[1]), w1 = cat8(fc.b[0], fc.b[1]);                              \
            _Pragma("unroll") for (int j = 0; j < 4; ++j) {                                                     \
                if (RD) { q_read(p, fn, j < 2 ? 2 * j : j + 2, sn, xb, wb); if (j < 2) q_read(p, fn, 2 * j + 1, sn, xb, wb); } \
                acc[j] = MFMA32((j & 1) ? w1 : w0, fc.x[j >> 1], acc[j]); PIN(acc[j]);                          \
                if (j == 0) { H0; } if (j == 1) { H1; } if (j == 2) { H2; } if (j == 3) { H3; }                 \
                SBAR();                                                                                         \
            }                                                                                                   \
        }
        constexpr unsigned W0 = S4, W1 = S2, W2 = S2 + 16384;
        u64 ep2; unsigned kvc;
        asm volatile("global_load_dwordx2 %0, %1, %2 sc1" : "=v"(ep2) : "v"(vzero), "s"(epoch) : "memory");
        Q_STEP(g0, g1, 1, S0, W0, true, pj_x(p, 2, S1), pj_x(p, 3, S1); XADV(), KVST(0), KVST(1))
        Q_STEP(g1, g0, 2, S0, W0, true, pj_x(p, 0, S3), pj_x(p, 1, S3), KVST(2), )
        Q_STEP(g0, g1, 3, S0, W0, true, pj_x(p, 2, S3), pj_x(p, 3, S3); XADV(), KVST(3), )
        SYNC_N(8);
        Q_STEP(g1, g0, 0, S1, W1, true, pj_x(p, 0, S0), , pj_x(p, 1, S0), )
        Q_STEP(g0, g1, 1, S1, W1, true, pj_x(p, 2, S0), , pj_x(p, 3, S0); XADV(), )
        Q_STEP(g1, g0, 2, S1, W1, true, pj_w(p, 0, W0), , KVST(4), )
        Q_STEP(g0, g1, 3, S1, W1, true, pj_w(p, 1, W0); WADV(), , KVST(5), )
        SYNC_N(8);
        PIN(ep2);
        if (w == 0) {
            unsigned spins = 0;
            while (ep2 != ep + 1 && ++spins < SPIN_CAP) { __builtin_amdgcn_s_sleep(2); ep2 = __hip_atomic_load(epoch, RLX_AGENT); }
        }
        KVSIG(0)
        Q_STEP(g1, g0, 0, S3, W2, true, pj_x(p, 0, S1), , pj_x(p, 1, S1), )
        Q_STEP(g0, g1, 1, S3, W2, true, pj_x(p, 2, S1), , pj_x(p, 3, S1); XADV(), )
        Q_STEP(g1, g0, 2, S3, W2, true, pj_w(p, 0, W1), , KVST(6), )
        Q_STEP(g0, g1, 3, S3, W2, true, pj_w(p, 1, W1); WADV(), , KVST(7), )
        SYNC_N(9);
        Q_STEP(g1, g0, 0, S0, W0, true, pj_x(p, 0, S3), , pj_x(p, 1, S3), )
        Q_STEP(g0, g1, 1, S0, W0, true, pj_x(p, 2, S3), , pj_x(p, 3, S3); XADV(), )
        Q_STEP(g1, g0, 2, S0, W0, true, pj_w(p, 0, W2), , KVST(8), )
        Q_STEP(g0, g1, 3, S0, W0, true, pj_w(p, 1, W2); WADV(), , KVST(9), )
        SYNC_N(8);
        KVSIG(1)
        Q_STEP(g1, g0, 0, S1, W1, true, pj_x(p, 0, S0), , pj_x(p, 1, S0), )
        Q_STEP(g0, g1, 1, S1, W1, true, pj_x(p, 2, S0), , pj_x(p, 3, S0); XADV(), )
        Q_STEP(g1, g0, 2, S1, W1, true, pj_w(p, 0, W0), , KVST(10), )
        Q_STEP(g0, g1, 3, S1, W1, true, pj_w(p, 1, W0); WADV(), , KVST(11), )
        SYNC_N(9);
        Q_STEP(g1, g0, 0, S3, W2, true, pj_x(p, 0, S1), , pj_x(p, 1, S1), )
        asm volatile("global_load_dword %0, %1, %2 sc1" : "=v"(kvc) : "v"((unsigned)(C_KV + h * 4) * 128u), "s"(ws + WS_FLAG + 8192) : "memory");
        Q_STEP(g0, g1, 1, S3, W2, true, pj_x(p, 2, S1), , pj_x(p, 3, S1), )
        Q_STEP(g1, g0, 2, S3, W2, true, pj_w(p, 0, W1), , KVST(12), )
        Q_STEP(g0, g1, 3, S3, W2, true, pj_w(p, 1, W1), , KVST(13), )
        SYNC_N(6);
        KVSIG(2)
        PIN(kvc);
        if (w == 0) {
            if (rfl(kvc) < 8u) wait_ge(ctr + (C_KV + h * 4) * 32, 8u);
            asm volatile("buffer_inv sc1" ::: "memory");
        }
        Q_STEP(g1, g0, 0, S0, W0, true, , , , )
        Q_STEP(g0, g1, 1, S0, W0, true, , , , )
        Q_STEP(g1, g0, 2, S0, W0, true, KVST(14), , WO_ST(0), )
        Q_STEP(g0, g1, 3, S0, W0, true, KVST(15), , WO_ST(1), )
        SYNC_N(4);
        Q_STEP(g1, g0, 0, S1, W1, true, at_load_tile(c), , at_load_tile(c), )
        Q_STEP(g0, g1, 1, S1, W1, true, , , , )
        Q_STEP(g1, g0, 2, S1, W1, true, , , , )
        Q_STEP(g0, g1, 3, S1, W1, true, , , , )
        SYNC_LDS();
        Q_STEP(g1, g0, 0, 0u, 0u, false, , , , )
        ST(5)
        asm volatile("" :: "v"(p.dummy));
#pragma unroll
        for (int tb = 0; tb < 2; ++tb) {
            const int tk = tq * 2 + tb;
            char* xq = lds + (tk < 4 ? S2 + tk * 8192 : S4 + (tk - 4) * 8192) + l16;
#pragma unroll
            for (int db = 0; db < 2; ++db)
#pragma unroll
                for (int sp = 0; sp < 2; ++sp) {
                    const int sfr = (dh * 2 + db) * 2 + sp;
                    const bf16x8 v = pack8(acc[tb * 2 + db], sp, QSCALE);
                    *(bf16x8*)(xq + sfr * 1024) = v;
                    if (tq >= 2) *(bf16x8*)(ws + WS_Q + ((size_t)b << 15) + (tk - 4) * 8192 + sfr * 1024 + l16) = v;
                }
        }
        if (!grp) { at_load_tile(c); at_load_tile(c); }
    }
    }
    const int n1 = 2 * (bi + 1);
    const int blk1 = grp ? bj : bi;
    SYNC_N(8);
    if (tid < 2) __hip_atomic_fetch_add(ctr + (tid ? C_WO : C_KV + h * 4 + 3) * 32, 1u, RLX_AGENT);
    c.kvoff = (C_KV + h * 4 + 1) * 128; c.kvneed = 32 - 2 * bi - 8 + 3; c.kvc = 0;
#pragma unroll
    for (int s = 0; s < 8; ++s) c.qf[s] = lds_rd128(c.lb + (grp ? 131072 : 65536) + rg * 8192 + s * 1024 + l16);
    c.negm = f32x16{}; PIN(c.negm);
#pragma unroll
    for (int i = 0; i < 4; ++i) c.o[i] = f32x16{};
    c.mref = 0.f; c.lsum = 0.f;
    SB sA, sB;
    {
        const unsigned ks_ = c.lb + l16;
#pragma unroll
        for (int s = 0; s < 8; ++s) {
            const bf16x8 k0 = lds_rd128(ks_ + s * 1024), k1 = lds_rd128(ks_ + 8192 + s * 1024);
            sA.a = MFMA32(k0, c.qf[s], s ? sA.a : f32x16{});
            sA.b = MFMA32(k1, c.qf[s], s ? sA.b : f32x16{});
        }
#pragma unroll
        for (int i = 0; i < NPRE; ++i) c.fr[i] = lds_rd128(ks_ + 16384 + kitem(i));
        at_mask(c, sA, 0, blk1);
        float ma = MX3(sA.a[0], sA.a[1], sA.b[0]);
        ma = __builtin_fmaxf(ma, sA.b[1]);
#pragma unroll
        for (int r = 2; r < 16; r += 2) { ma = MX3(ma, sA.a[r], sA.a[r + 1]); ma = MX3(ma, sA.b[r], sA.b[r + 1]); }
        at_check(c, sA, ma, true);
    }
    ST(6)
    for (int t = 0; t < n1 - 2; t += 2) {
        at_step<0, 1>(c, sA, sB, 1, 1, blk1);
        at_step<0, 1>(c, sB, sA, 1, 1, blk1);
    }
    at_step<1, 2>(c, sA, sB, 1, 1 + grp, blk1);
    at_step<2, 2>(c, sB, sA, 1 + grp, 2, bj);
    for (int t = n1; t < 16; t += 2) {
        at_step<0, 2>(c, sA, sB, 2, 2, bj);
        at_step<0, 2>(c, sB, sA, 2, 2, bj);
    }
    unsigned ticket = 0;
    if (w == 0 && c.l16 == 0) ticket = __hip_atomic_fetch_add(ctr + (C_TICK + xcd) * 32, 1u, RLX_AGENT);
    {
        typedef const void* const __attribute__((address_space(4)))* kargs_t;
        kargs_t ka = (kargs_t)__builtin_amdgcn_kernarg_segment_ptr();
        asm volatile("" : "+s"(ka));
        const char* ub = (const char*)ka[6] + (size_t)(w * 256) * 1024 + (xcd * 64 + idx * 2) * 2;
        const unsigned vo = c.l16 * 64;
#pragma unroll
        for (int k = 0; k < 4; ++k) {
            unsigned keep;
            asm volatile("s_mov_b32 %0, m0\n\ts_mov_b32 m0, %3\n\ts_nop 0\n\tglobal_load_lds_dword %1, %2\n\ts_mov_b32 m0, %0"
                         : "=&s"(keep) : "v"(vo), "s"(ub + k * 65536), "s"(c.lb + T_U2 + w * 1024 + k * 256) : "memory");
        }
    }
    at_x<false>(c, sA, sB);
    at_bar<false>(c);
    tail_part(c, sB, lds, ticket DIAGARGS);
}

extern "C" void kernel_launch(void* const* d_in, const int* in_sizes, int n_in, void* d_out, int out_size, void* d_ws,
                              size_t ws_size, hipStream_t stream) {
    (void)in_sizes; (void)n_in; (void)out_size; (void)ws_size;
    const unsigned short* const* in = (const unsigned short* const*)d_in;
    mega_kernel<<<256, 512, 0, stream>>>(in[0], in[1], in[2], in[3], in[4], in[5], in[6], in[7], in[8], in[9], in[10], in[11],
                                         in[12], in[13], in[14], (char*)d_ws, (unsigned short*)d_out);
}
```

```cpp
#include <hip/hip_runtime.h>
#include <hip/hip_bf16.h>
#include <stdint.h>

typedef __bf16 bf16x8 __attribute__((ext_vector_type(8)));
typedef __bf16 bf16x2 __attribute__((ext_vector_type(2)));
typedef short s16x4 __attribute__((ext_vector_type(4)));
typedef float f32x16 __attribute__((ext_vector_type(16)));
typedef float f32x4 __attribute__((ext_vector_type(4)));
typedef float f32x2 __attribute__((ext_vector_type(2)));
typedef unsigned u32x4 __attribute__((ext_vector_type(4)));
typedef unsigned long long u64;
typedef const unsigned short* __restrict__ cbf;
typedef __attribute__((address_space(1))) unsigned gu32;
typedef __attribute__((address_space(1))) u64 gu64;
#define LAS __attribute__((address_space(3)))
#define DI __device__ __forceinline__
#define MFMA32(a, b, c) __builtin_amdgcn_mfma_f32_32x32x16_bf16((a), (b), (c), 0, 0, 0)
#define MFMA16(a, b, c) __builtin_amdgcn_mfma_f32_16x16x32_bf16((a), (b), (c), 0, 0, 0)
#define RLX_AGENT __ATOMIC_RELAXED, __HIP_MEMORY_SCOPE_AGENT
#define SBAR() __builtin_amdgcn_sched_barrier(0)
#define PIN(x) asm volatile("" : "+v"(x))
#define MX3(a, b, c) __builtin_fmaxf(__builtin_fmaxf((a), (b)), (c))

constexpr int DM = 512;
constexpr float QSCALE = 0.08838834764831845f * 1.4426950408889634f;
#define THRV 40.0f
constexpr float THR = THRV;
constexpr unsigned SPIN_CAP = 1u << 22;

constexpr size_t WS_Q = 0;
constexpr size_t WS_UT = 8u << 20;
constexpr size_t WS_KV = 16u << 20;
constexpr size_t WS_CTX = 48u << 20;
constexpr size_t WS_WO = 64u << 20;
constexpr size_t WS_FLAG = 72u << 20;
constexpr int C_KV = 0, C_CTX = 128, C_WO = 144, C_TICK = 145, C_NUM = 153;

DI unsigned cvtpk(float lo, float hi) { f32x2 v = {lo, hi}; bf16x2 b = __builtin_convertvector(v, bf16x2); return __builtin_bit_cast(unsigned, b); }
DI float bf2f(unsigned short b) { return __uint_as_float(((unsigned)b) << 16); }
DI float bfround(float x) { __bf16 b = (__bf16)x; return (float)b; }
DI unsigned rfl(unsigned x) { return __builtin_amdgcn_readfirstlane(x); }
DI bf16x8 pack2(f32x4 x, f32x4 y) {
    u32x4 w = {cvtpk(x[0], x[1]), cvtpk(x[2], x[3]), cvtpk(y[0], y[1]), cvtpk(y[2], y[3])};
    return __builtin_bit_cast(bf16x8, w);
}
DI float gsum(float x) {
    auto s = __builtin_amdgcn_permlane16_swap(__float_as_uint(x), __float_as_uint(x), false, false);
    x = __uint_as_float(s[0]) + __uint_as_float(s[1]);
    auto t = __builtin_amdgcn_permlane32_swap(__float_as_uint(x), __float_as_uint(x), false, false);
    return __uint_as_float(t[0]) + __uint_as_float(t[1]);
}
DI float gmax(float x) {
    auto s = __builtin_amdgcn_permlane16_swap(__float_as_uint(x), __float_as_uint(x), false, false);
    x = fmaxf(__uint_as_float(s[0]), __uint_as_float(s[1]));
    auto t = __builtin_amdgcn_permlane32_swap(__float_as_uint(x), __float_as_uint(x), false, false);
    return fmaxf(__uint_as_float(t[0]), __uint_as_float(t[1]));
}
DI bf16x8 lds_rd128(unsigned addr) { return *(const LAS bf16x8*)(uintptr_t)addr; }
DI s16x4 lds_tr(unsigned addr) { return __builtin_amdgcn_ds_read_tr16_b64_v4i16((LAS s16x4*)(uintptr_t)addr); }
DI bf16x8 cat8(s16x4 lo, s16x4 hi) { return __builtin_bit_cast(bf16x8, __builtin_shufflevector(lo, hi, 0, 1, 2, 3, 4, 5, 6, 7)); }

DI void glds1(const void* sbase, unsigned voff, unsigned m0v) {
    unsigned keep;
    asm volatile("s_mov_b32 %0, m0\n\ts_mov_b32 m0, %3\n\ts_nop 0\n\tglobal_load_lds_dwordx4 %1, %2\n\ts_mov_b32 m0, %0"
                 : "=&s"(keep) : "v"(voff), "s"(sbase), "s"(m0v) : "memory");
}
template <int OFF> DI void glds2m(const void* sbase, unsigned voff, unsigned m0v, int left) {
    unsigned keep;
    asm volatile("s_mov_b32 %0, m0\n\ts_mov_b32 m0, %3\n\ts_cmp_gt_i32 %4, 0\n\ts_cselect_b64 exec, -1, 0\n\t"
                 "global_load_lds_dwordx4 %1, %2 offset:%5 sc1\n\tglobal_load_lds_dwordx4 %1, %2 offset:%6 sc1\n\ts_mov_b64 exec, -1\n\ts_mov_b32 m0, %0"
                 : "=&s"(keep) : "v"(voff), "s"(sbase), "s"(m0v), "s"(left), "i"(OFF), "i"(OFF + 1024) : "memory", "scc");
}
DI void glds4(const void* sbase, unsigned voff, unsigned m0v) {
    unsigned keep;
    asm volatile("s_mov_b32 %0, m0\n\ts_mov_b32 m0, %3\n\ts_nop 0\n\t"
                 "global_load_lds_dwordx4 %1, %2\n\tglobal_load_lds_dwordx4 %1, %2 offset:1024\n\t"
                 "global_load_lds_dwordx4 %1, %2 offset:2048\n\tglobal_load_lds_dwordx4 %1, %2 offset:3072\n\ts_mov_b32 m0, %0"
                 : "=&s"(keep) : "v"(voff), "s"(sbase), "s"(m0v) : "memory");
}
DI void st16(char* ubase, unsigned voff, bf16x8 v) {
    __amdgpu_buffer_rsrc_t r = __builtin_amdgcn_make_buffer_rsrc(ubase, (short)0, 0x7fffffff, 0x00020000);
    __builtin_amdgcn_raw_buffer_store_b128(__builtin_bit_cast(u32x4, v), r, voff, 0, 16);
}
DI f32x4 mfma_c(bf16x8 a, bf16x8 b, const f32x4& cc) {
    f32x4 d;
    asm volatile("v_mfma_f32_16x16x32_bf16 %0, %1, %2, %3" : "=&v"(d) : "v"(a), "v"(b), "v"(cc));
    return d;
}
DI void mfma_acc(f32x4& acc, bf16x8 a, bf16x8 b) { asm volatile("v_mfma_f32_16x16x32_bf16 %0, %1, %2, %0" : "+v"(acc) : "v"(a), "v"(b)); }
#define MFMA_DONE() { SBAR(); asm volatile("s_nop 7"); SBAR(); }
#define SYNC_N(n) asm volatile("s_waitcnt vmcnt(" #n ") lgkmcnt(0)\n\ts_barrier" ::: "memory")
#define SYNC_ALL() SYNC_N(0)
#define SYNC_LDS() asm volatile("s_waitcnt lgkmcnt(0)\n\ts_barrier" ::: "memory")
DI void atomic_inc1(char* ws, unsigned id, unsigned l16, unsigned mask) {
    const unsigned one = 1;
    asm volatile("s_mov_b32 exec_lo, %3\n\ts_mov_b32 exec_hi, 0\n\tglobal_atomic_add %0, %1, %2\n\ts_mov_b64 exec, -1"
                 :: "v"(l16 * 8 + id * 128), "v"(one), "s"(ws + WS_FLAG + 8192), "s"(mask) : "memory");
}
DI void wait_ge(gu32* p, unsigned want) {
    unsigned spins = 0;
    while (rfl(__hip_atomic_load(p, RLX_AGENT)) < want && ++spins < SPIN_CAP) __builtin_amdgcn_s_sleep(2);
}
DI u64 ack_of(u64 e, int wg) { return (e ^ 0x9E3779B97F4A7C15ull) + (u64)(wg + 1) * 0x100000001B3ull; }

#define ST(i)
#define ST_END()

DI unsigned opaque(unsigned x) { asm volatile("" : "+s"(x)); return x; }
struct PJ {
    const char* xs; const char* w1; const char* w2;
    const char* wp; unsigned winc, dummy;
    unsigned lb, xoff0, xoff1, woff[2], xdst, wdst;
    unsigned xa[2], wa[4][2];
};
DI void pj_x(const PJ& c, int q, unsigned xb) { glds1(c.xs + q * 8192, (q & 1) ? c.xoff1 : c.xoff0, c.xdst + xb + q * 1024); }
DI void pj_w(const PJ& c, int q, unsigned wb) { glds1((q < 2 ? c.w1 : c.w2) + (q & 1) * 32768, c.woff[q & 1], c.wdst + wb + (q >> 1) * 16384 + (q & 1) * 1024); }
DI void pj_warm(PJ& c) {
    asm volatile("s_mov_b64 exec, 0xffffff\n\tglobal_load_dword %0, %1, off\n\ts_mov_b64 exec, -1" : "+v"(c.dummy) : "v"(c.wp) : "memory");
    c.wp += c.winc;
}
struct FW { s16x4 h[4][2]; };
DI bf16x8 fw(const FW& f, int i) { return cat8(f.h[i][0], f.h[i][1]); }
DI void kvw_read(const PJ& c, FW& f, int j, int s, unsigned wb) { f.h[j >> 1][j & 1] = lds_tr(c.wa[(j >> 1) & 1][j & 1] + wb + (j >> 2) * 16384 + s * 8192); }
DI void qw_read(const PJ& c, FW& f, int j, int s, unsigned wb) { f.h[j >> 1][j & 1] = lds_tr(c.wa[j >> 1][j & 1] + wb + s * 8192); }
DI bf16x8 x_read(const PJ& c, int tt, int s, unsigned xb) { return lds_rd128(c.xa[s] + xb + tt * 2048); }

#define NPREV 6
constexpr int NPRE = NPREV;
struct SB { f32x4 s[4][2]; };
struct AT {
    char* ws; const char* ldp; const char* qh;
    unsigned sign, sigid;
    unsigned kvoff; int kvneed; unsigned kvc;
    int h, bi, bj, grp, rg, qrel, w;
    unsigned lb, l16;
    unsigned ldoff, ldlim, ldbase;
    unsigned koff, voff;
    int taun, ldleft;
    bf16x8 qf[2][4], fr[8];
    unsigned pw[16];
    f32x4 o[8][2], negm[2];
    f32x4 ls[2];
    bf16x8 ones;
    unsigned s4[2];
    float mref[2];
};
DI void at_piece(AT& c, int q) {
    const unsigned m0v = c.ldbase + c.ldoff;
    if (q == 0) glds2m<0>(c.ldp, c.l16, m0v, c.ldleft); else glds2m<2048>(c.ldp, c.l16, m0v, c.ldleft);
}
DI void at_adv(AT& c, int on = 1) { c.ldp += on * 32768; c.ldoff += on * 16384; if (c.ldoff >= c.ldlim) c.ldoff = 0; c.ldleft -= on; }
DI void at_load_tile(AT& c) { glds4(c.ldp, c.l16, c.ldbase + c.ldoff); at_adv(c); }
DI unsigned kitem(int j) { return ((j & 3) * 4 + (j >> 2)) * 1024; }
DI unsigned vitem(int j) { return ((j & 7) * 2 + (j >> 3)) * 1024; }
#define SEL(S, e) (S).s[((e) >> 4) * 2 + (((e) >> 2) & 1)][((e) >> 3) & 1][(e) & 3]

DI void at_mask(AT& c, SB& s, int tau, int blk) {
    const int row0 = blk * 128 + c.rg * 32;
    if (tau * 64 + 63 > row0) {
        MFMA_DONE()
        const float dq = (float)(row0 + c.qrel - tau * 64);
#pragma unroll
        for (int kt = 0; kt < 4; ++kt)
#pragma unroll
            for (int qt = 0; qt < 2; ++qt)
#pragma unroll
                for (int e = 0; e < 4; ++e)
                    s.s[kt][qt][e] = __builtin_fmaf(__builtin_fminf(dq - (float)(kt * 16 + e - qt * 16), 0.f), 1e30f, s.s[kt][qt][e]);
    }
#pragma unroll
    for (int i = 0; i < 8; ++i) PIN(s.s[i >> 1][i & 1]);
}
DI void at_check(AT& c, SB& s, float ma, bool first) {
    if (__builtin_expect(first || __any(ma > THR), 0)) {
        MFMA_DONE()
#pragma unroll
        for (int qt = 0; qt < 2; ++qt) {
            float mx = MX3(s.s[0][qt][0], s.s[0][qt][1], s.s[0][qt][2]);
            mx = fmaxf(mx, s.s[0][qt][3]);
#pragma unroll
            for (int kt = 1; kt < 4; ++kt) { mx = MX3(mx, s.s[kt][qt][0], s.s[kt][qt][1]); mx = MX3(mx, s.s[kt][qt][2], s.s[kt][qt][3]); }
            mx = gmax(mx);
            const float dl = first ? mx : __builtin_fmaxf(mx, 0.f);
            c.mref[qt] += dl; PIN(c.mref[qt]);

#pragma unroll
            for (int kt = 0; kt < 4; ++kt) s.s[kt][qt] -= dl;
            c.negm[qt] = f32x4{-c.mref[qt], -c.mref[qt], -c.mref[qt], -c.mref[qt]};
            PIN(c.negm[qt]);
            if (!first) {
                const float fs = __builtin_amdgcn_exp2f(-dl);
                c.ls[qt] *= fs;
#pragma unroll
                for (int d = 0; d < 8; ++d) c.o[d][qt] *= fs;
            }
        }
        asm volatile("s_nop 1");
    }
#pragma unroll
    for (int i = 0; i < 8; ++i) PIN(s.s[i >> 1][i & 1]);
}
template <bool QK> DI void at_x(AT& c, SB& sc, SB& sn) {
    const unsigned ks_ = c.lb + 98304 + c.koff + c.l16, vs_ = c.lb + c.voff + c.l16;
    float e0 = 0.f, e1 = 0.f;
#pragma unroll
    for (int i = 0; i < 16; ++i) {
        const int n = i + NPRE, kt = i & 3, ds = i >> 2;
        if (n < 16) { if (QK) c.fr[n & 7] = lds_rd128(ks_ + kitem(n)); }
        else c.fr[n & 7] = lds_rd128(vs_ + vitem(n - 16));
        if (QK) { if (ds) mfma_acc(sn.s[kt][0], c.fr[i & 7], c.qf[0][ds]); else sn.s[kt][0] = mfma_c(c.fr[i & 7], c.qf[0][0], c.negm[0]); }
        if (i > 0) { c.pw[i - 1] = cvtpk(e0, e1); PIN(c.pw[i - 1]); }
        e0 = __builtin_amdgcn_exp2f(SEL(sc, 2 * i)); PIN(e0);
        SBAR();
        if (QK) { if (ds) mfma_acc(sn.s[kt][1], c.fr[i & 7], c.qf[1][ds]); else sn.s[kt][1] = mfma_c(c.fr[i & 7], c.qf[1][0], c.negm[1]); }
        e1 = __builtin_amdgcn_exp2f(SEL(sc, 2 * i + 1)); PIN(e1);
        SBAR();
    }
    c.pw[15] = cvtpk(e0, e1); PIN(c.pw[15]);
}
template <bool POLL> DI void at_bar(AT& c) {
    if (POLL) asm volatile("s_waitcnt vmcnt(0)\n\ts_barrier" ::: "memory");
    else asm volatile("s_waitcnt vmcnt(0)\n\ts_barrier" ::: "memory");
    if (c.sign) {
        if (c.w == 0) atomic_inc1(c.ws, c.sigid, c.l16, c.sign);
        c.sign = 0;
    }
    PIN(c.kvc);
    if (!POLL) return;
    const bool need = c.ldleft - 2 * c.grp <= c.kvneed && c.kvneed > 3;
    if (need && c.w == 0) {
        if (rfl(c.kvc) < 8u) wait_ge((gu32*)(c.ws + WS_FLAG + 8192 + c.kvoff), 8u);
    }
    c.kvoff += need ? 128 : 0; c.kvneed -= need ? 8 : 0;
    asm volatile("global_load_dword %0, %1, %2 sc1" : "=v"(c.kvc) : "v"(c.kvoff), "s"(c.ws + WS_FLAG + 8192) : "memory");
}
#define PFRAG(u, qt) __builtin_bit_cast(bf16x8, u32x4{c.pw[((u) * 2 + (qt)) * 4], c.pw[((u) * 2 + (qt)) * 4 + 1], c.pw[((u) * 2 + (qt)) * 4 + 2], c.pw[((u) * 2 + (qt)) * 4 + 3]})
template <bool NEXT, bool QLD> DI float at_y(AT& c, SB& sn, int inc, int blkn, int two) {
    if (NEXT) at_bar<true>(c);
    if (QLD) {
#pragma unroll
        for (int s = 0; s < 8; ++s) c.qf[s >> 2][s & 3] = *(const bf16x8*)(c.qh + c.rg * 8192 + s * 1024 + c.l16);
    }
    if (NEXT) at_mask(c, sn, c.taun, blkn);
    const unsigned vs_ = c.lb + c.voff + c.l16;
    c.koff = (c.koff + inc * 16384) & 65535;
    const unsigned ks_ = c.lb + 98304 + c.koff + c.l16;
    float ma = -__builtin_inff();
#pragma unroll
    for (int i = 0; i < 16; ++i) {
        const int n = i + NPRE, dt = i & 7, u = i >> 3, lq = (i >> 2) & 1;
        if (n < 16) c.fr[n & 7] = lds_rd128(vs_ + vitem(n));
        else if (NEXT) c.fr[n & 7] = lds_rd128(ks_ + kitem(n - 16));
        mfma_acc(c.o[dt][0], c.fr[i & 7], PFRAG(u, 0));
        if ((i & 3) == 3) mfma_acc(c.ls[lq], c.ones, PFRAG(u, lq));
        SBAR();
        mfma_acc(c.o[dt][1], c.fr[i & 7], PFRAG(u, 1));
        if (NEXT) { ma = MX3(ma, SEL(sn, 2 * i), SEL(sn, 2 * i + 1)); PIN(ma); }
        if ((i & 3) == 1) {
            const int q = i >> 2;
            if (NEXT && !QLD && (q < 2 || two)) { at_piece(c, q & 1); if (q & 1) at_adv(c); }
        }
        SBAR();
    }
    return ma;
}
template <int KIND> DI void at_step(AT& c, SB& sc, SB& sn, int incv, int inck, int blkn, int two) {
    at_x<true>(c, sc, sn);
    const float ma = at_y<true, KIND == 1>(c, sn, inck, blkn, two);
    c.voff += incv * 16384; if (c.voff >= 98304) c.voff -= 98304;
    c.taun += inck;
    if constexpr (KIND == 2) {
        if (!c.grp) {
            MFMA_DONE()
#pragma unroll
            for (int qt = 0; qt < 2; ++qt) {
                const float inv = 1.0f / c.ls[qt][0];
                char* dst = c.ws + WS_CTX + ((size_t)((c.bi * 8 + c.rg * 2 + qt) * 128 + c.h * 4) << 10);
#pragma unroll
                for (int sp = 0; sp < 4; ++sp) st16(dst + sp * 1024, c.l16, pack2(c.o[2 * sp][qt] * inv, c.o[2 * sp + 1][qt] * inv));
#pragma unroll
                for (int k = 0; k < 8; ++k) c.o[k][qt] = f32x4{};
                c.mref[qt] = 0.f; c.ls[qt] = f32x4{}; PIN(c.mref[qt]);
            }
        }
        PIN(c.mref[0]); PIN(c.mref[1]);
        c.sigid = C_CTX + c.bi; c.sign = 1;
        at_check(c, sn, ma, !c.grp);
    } else {
        at_check(c, sn, ma, false);
    }
    if constexpr (KIND == 1) {
#pragma unroll
        for (int s = 0; s < 8; ++s) PIN(c.qf[s >> 2][s & 3]);
#pragma unroll
        for (int q = 0; q < 4; ++q) { at_piece(c, q & 1); if (q & 1) at_adv(c); }
        if (!c.grp) {
            c.negm[0] = f32x4{}; c.negm[1] = f32x4{}; PIN(c.negm[0]); PIN(c.negm[1]); asm volatile("s_nop 1");
        }
    }
}

struct cf { float r, i; };
DI cf cmul(cf a, cf b) { return cf{a.r * b.r - a.i * b.i, a.r * b.i + a.i * b.r}; }
DI cf cexp_scaled(float a2, float brev, float v) {
    const float e = __builtin_amdgcn_exp2f(a2 * v);
    const float ph = __builtin_amdgcn_fractf(brev * v);
    return cf{e * __builtin_amdgcn_cosf(ph), e * __builtin_amdgcn_sinf(ph)};
}
constexpr int GROW = 528;
constexpr int UCOPY = 4160;
constexpr int T_U2 = 64;
constexpr int T_PRM = 8448;
constexpr int T_RED = 12032;
constexpr int T_UC = T_RED + 49152;
constexpr int T_KP = T_UC + 2 * 8 * UCOPY;
constexpr int T_KD = T_KP + 2 * 12288;
constexpr int KDB = 2592;
constexpr int T_CNT = T_KD + 4 * KDB;
static_assert(T_CNT + 16 <= 163840 && T_CNT >= T_RED + 8 * 9216, "lds");
DI void role_sync(LAS unsigned* cnt, unsigned target, int lane) {
    asm volatile("s_waitcnt lgkmcnt(0)" ::: "memory");
    if (lane == 0) __hip_atomic_fetch_add(cnt, 1u, __ATOMIC_RELAXED, __HIP_MEMORY_SCOPE_WORKGROUP);
    while (__hip_atomic_load(cnt, __ATOMIC_RELAXED, __HIP_MEMORY_SCOPE_WORKGROUP) < target) __builtin_amdgcn_s_sleep(1);
    asm volatile("" ::: "memory");
}
DI void hilo_store(char* kp, int c8, const float (&kv)[8]) {
    unsigned hi[4], lo[4];
#pragma unroll
    for (int e = 0; e < 4; ++e) {
        hi[e] = cvtpk(kv[2 * e], kv[2 * e + 1]);
        lo[e] = cvtpk(kv[2 * e] - __uint_as_float(hi[e] << 16), kv[2 * e + 1] - __uint_as_float(hi[e] & 0xffff0000u));
    }
    const int off = (c8 & 3) * 1536 + ((c8 >> 2) + 32) * 16;
    *(u32x4*)(kp + off) = u32x4{hi[0], hi[1], hi[2], hi[3]};
    *(u32x4*)(kp + 6144 + off) = u32x4{lo[0], lo[1], lo[2], lo[3]};
}
#define ODV 4
constexpr int OD = ODV;

#define DIAGARGS
#define DIAGPARAMS
DI void tail_part(AT& c, SB& sdummy, char* lds, unsigned ticket DIAGPARAMS) {
    unsigned lane_;
    asm volatile("v_mbcnt_lo_u32_b32 %0, -1, 0\n\tv_mbcnt_hi_u32_b32 %0, -1, %0" : "=v"(lane_));
    typedef const void* const __attribute__((address_space(4)))* kargs_t;
    kargs_t ka = (kargs_t)__builtin_amdgcn_kernarg_segment_ptr();
    asm volatile("" : "+s"(ka));
    unsigned short* __restrict__ out = (unsigned short*)ka[16];
    char* ws = c.ws;
    const int lane = lane_, w = c.w, tid = w * 64 + lane, r32 = lane & 31, hh = lane >> 5, grp = c.grp, rg = c.rg, h = c.h, bj = c.bj;
    const unsigned l16 = lane * 16;
    const int b = blockIdx.x, xcd = b & 7, idx = b >> 3;
    gu32* ctr = (gu32*)(ws + WS_FLAG + 8192);
    const int d0 = xcd * 64 + idx * 2;
    unsigned rdy;
    ticket = rfl(ticket);
    const int mb0 = (ticket < 16 ? 0 : 16) + xcd * 2 + ((ticket >> 3) & 1);
    asm volatile("global_load_dword %0, %1, %2 sc1" : "=v"(rdy) : "v"((unsigned)((lane & 1) ? C_CTX + (mb0 >> 1) : C_WO) * 128u), "s"(ws + WS_FLAG + 8192) : "memory");
    SBAR();
    at_y<false, false>(c, sdummy, 0, bj, 0);
    MFMA_DONE()
    ST(7)
    LAS unsigned* cnts = (LAS unsigned*)(uintptr_t)(c.lb + T_CNT);
    {
        const float l0 = c.ls[0][0], l1 = c.ls[1][0];
        SYNC_LDS();
        char* mine = lds + T_RED + w * 9216;
#pragma unroll
        for (int d = 0; d < 4; ++d)
#pragma unroll
            for (int qt = 0; qt < 2; ++qt) {
                const f32x4 oa = c.o[d][qt], ob = c.o[4 + d][qt];
                *(f32x4*)(mine + (d * 2 + qt) * 1024 + l16) = grp ? oa : ob;
            }
        *(f32x4*)(mine + 8192 + l16) = f32x4{c.mref[0], l0, c.mref[1], l1};
        PIN(c.s4[0]); PIN(c.s4[1]);
        *(unsigned*)(lds + T_PRM + tid * 4) = c.s4[0];
        if (tid < 258) *(unsigned*)(lds + T_PRM + 2048 + tid * 4) = c.s4[1];
        asm volatile("s_waitcnt vmcnt(0)" : "+v"(rdy) :: "memory");
        if (w == 0) {
            if (rfl(rdy) < 256u) wait_ge(ctr + C_WO * 32, 256u);
            const bool ok = __builtin_amdgcn_readlane(rdy, 1) >= 32u;
            if (lane == 0) { cnts[0] = 0; cnts[1] = 0; cnts[2] = ticket | (ok ? 256u : 0u); cnts[3] = 0; }
            if (lane < 16) *(unsigned*)(lds + lane * 4) = 0u;
        }
        SYNC_LDS();
        const __amdgpu_buffer_rsrc_t ru = __builtin_amdgcn_make_buffer_rsrc(ws + WS_UT + (size_t)(d0 >> 1) * 8192, (short)0, 8192, 0x00020000);
        const u32x4 uv = __builtin_amdgcn_raw_buffer_load_b128(ru, tid * 16, 0, 16);
        const char* oth = lds + T_RED + (w ^ 4) * 9216;
        const f32x4 ml = *(const f32x4*)(oth + 8192 + l16);
#pragma unroll
        for (int qt = 0; qt < 2; ++qt) {
            const float mo = ml[2 * qt], lo = ml[2 * qt + 1], mm = c.mref[qt], lm = qt ? l1 : l0;
            const float m = fmaxf(mm, mo);
            const float fa = __builtin_amdgcn_exp2f(mm - m), fb = __builtin_amdgcn_exp2f(mo - m);
            const float inv = 1.0f / (lm * fa + lo * fb);
            const float ca = fa * inv, cb = fb * inv;
            char* dst = ws + WS_CTX + ((size_t)((bj * 8 + rg * 2 + qt) * 128 + h * 4 + (grp ? 2 : 0)) << 10);
            f32x4 r[4];
#pragma unroll
            for (int d = 0; d < 4; ++d) {
                const f32x4 oa = c.o[d][qt], ob = c.o[4 + d][qt];
                r[d] = (grp ? ob : oa) * ca + *(const f32x4*)(oth + (d * 2 + qt) * 1024 + l16) * cb;
            }
            st16(dst, l16, pack2(r[0], r[1])); st16(dst + 1024, l16, pack2(r[2], r[3]));
        }
        *(u32x4*)(lds + T_U2 + tid * 16) = uv;
    }
    const unsigned tk = rfl(cnts[2]);
    SYNC_LDS();
    ST(8)
#define CTX_SIGNAL() { asm volatile("s_waitcnt vmcnt(0)" ::: "memory");                                                          \
        if (lane == 0 && __hip_atomic_fetch_add(cnts + 3, 1u, __ATOMIC_RELAXED, __HIP_MEMORY_SCOPE_WORKGROUP) == 7u)             \
            __hip_atomic_fetch_add(ctr + (C_CTX + bj) * 32, 1u, RLX_AGENT); }
    if (!grp) {
        const int mb = ((tk & 255) < 16 ? 0 : 16) + xcd * 2 + ((tk >> 3) & 1), nb = tk & 7;
        const bool early = tk >= 256;
        if (!early) {
            CTX_SIGNAL()
            if (w == 0) wait_ge(ctr + (C_CTX + (mb >> 1)) * 32, 32u);
            role_sync(cnts, 4, lane);
        }
        __builtin_amdgcn_fence(__ATOMIC_ACQUIRE, "wavefront");
        ST(9)
        const __amdgpu_buffer_rsrc_t ra = __builtin_amdgcn_make_buffer_rsrc(ws + WS_CTX + ((size_t)((mb * 4) * 128 + w * 32) << 10), (short)0, 0x7fffffff, 0x00020000);
        const __amdgpu_buffer_rsrc_t rb_ = __builtin_amdgcn_make_buffer_rsrc(ws + WS_WO + ((size_t)((nb * 4) * 128 + w * 32) << 10), (short)0, 0x7fffffff, 0x00020000);
#define LDA(off) __builtin_bit_cast(bf16x8, __builtin_amdgcn_raw_buffer_load_b128(ra, l16, (off), 16))
#define LDB(off) __builtin_bit_cast(bf16x8, __builtin_amdgcn_raw_buffer_load_b128(rb_, l16, (off), 16))
        f32x4 acc[4][4];
#pragma unroll
        for (int i = 0; i < 16; ++i) acc[i >> 2][i & 3] = f32x4{};
        bf16x8 rga[OD][4], rgb[OD][4];
#pragma unroll
        for (int k = 0; k < OD; ++k)
#pragma unroll
            for (int i = 0; i < 4; ++i) { rga[k][i] = LDA(i * 131072 + k * 1024); rgb[k][i] = LDB(i * 131072 + k * 1024); }
        SBAR();
        if (early) {
            asm volatile("s_waitcnt vmcnt(%0)" :: "i"(8 * OD) : "memory");
            if (lane == 0 && __hip_atomic_fetch_add(cnts + 3, 1u, __ATOMIC_RELAXED, __HIP_MEMORY_SCOPE_WORKGROUP) == 7u)
                __hip_atomic_fetch_add(ctr + (C_CTX + bj) * 32, 1u, RLX_AGENT);
        }
        SBAR();
#pragma unroll 1
        for (int k0 = 0; k0 < 32 - OD; k0 += OD) {
#pragma unroll
            for (int k = 0; k < OD; ++k) {
#pragma unroll
                for (int i = 0; i < 16; ++i) acc[i >> 2][i & 3] = MFMA16(rgb[k][i & 3], rga[k][i >> 2], acc[i >> 2][i & 3]);
                SBAR();
                const int kn = (k0 + OD + k) * 1024;
#pragma unroll
                for (int i = 0; i < 4; ++i) { rga[k][i] = LDA(i * 131072 + kn); rgb[k][i] = LDB(i * 131072 + kn); }
                SBAR();
            }
        }
#pragma unroll
        for (int k = 0; k < OD; ++k)
#pragma unroll
            for (int i = 0; i < 16; ++i) acc[i >> 2][i & 3] = MFMA16(rgb[k][i & 3], rga[k][i >> 2], acc[i >> 2][i & 3]);
        ST(10)
#pragma unroll
        for (int i = 0; i < 4; ++i) {
            if (i != w) {
                const int slot = w < i ? w : w - 1;
#pragma unroll
                for (int t = 0; t < 4; ++t) *(f32x4*)(lds + T_RED + (i * 3 + slot) * 4096 + t * 1024 + l16) = acc[i][t];
            }
        }
        role_sync(cnts, early ? 4 : 8, lane);
        unsigned pk[8];
#pragma unroll
        for (int t = 0; t < 4; ++t) {
            f32x4 sm = w == 0 ? acc[0][t] : (w == 1 ? acc[1][t] : (w == 2 ? acc[2][t] : acc[3][t]));
#pragma unroll
            for (int sl = 0; sl < 3; ++sl) sm += *(const f32x4*)(lds + T_RED + (w * 3 + sl) * 4096 + t * 1024 + l16);
            pk[2 * t] = cvtpk(sm[0], sm[1]); pk[2 * t + 1] = cvtpk(sm[2], sm[3]);
        }
        unsigned short* orow = out + (size_t)(mb * 64 + w * 16 + (lane & 15)) * 1024 + nb * 64 + (lane >> 4) * 16;
        *(u32x4*)(orow) = u32x4{pk[0], pk[1], pk[2], pk[3]};
        *(u32x4*)(orow + 8) = u32x4{pk[4], pk[5], pk[6], pk[7]};
        ST_END()
        return;
    }
    const int t8 = tid - 256, ch = rg >> 1, wi = rg & 1, t7 = t8 & 127;
    LAS unsigned* cnt = cnts + 1;
    CTX_SIGNAL()
    {
        const unsigned short* pl = (const unsigned short*)(lds + T_PRM);
        const float dt = bfround(expf(bf2f(pl[1536 + ch])));
        const int i16 = lane & 15, g4 = lane >> 4;
        const int wx = (r32 >> 1) ^ (hh * 8);
        char* scr = lds + T_UC + rg * 4096;
        char* wr0 = scr + hh * 2048 + (r32 & 1) * 8;
        f32x4 res[3][2];
#pragma unroll
        for (int T = 0; T < 3; ++T) { res[T][0] = f32x4{}; res[T][1] = f32x4{}; }
#pragma unroll 1
        for (int z = 0; z < 2; ++z) {
#define PZ(i) bf2f(pl[(i) * 256 + ch * 128 + wi * 64 + z * 32 + r32])
            const float a2 = dt * PZ(0) * 1.4426950408889634f, brev = dt * PZ(1) * 0.15915494309189535f;
            const cf coef = cmul(cf{PZ(4), PZ(5)}, cf{dt * PZ(2), dt * PZ(3)});
            const cf e1 = cexp_scaled(a2, brev, 1.f), e8 = cexp_scaled(a2, brev, 8.f), e128 = cexp_scaled(a2, brev, 128.f);
            const cf e2 = cmul(e1, e1), e4 = cmul(e2, e2);
            f32x4 fb[4];
#pragma unroll
            for (int T = 0; T < 4; ++T) {
                const float vs = T == 0 ? (hh ? 64.f : 0.f) : T == 1 ? (hh ? 128.f : 0.f) : T == 2 ? (hh ? 512.f : 256.f) : (hh ? 2048.f : 1024.f);
                cf g = cexp_scaled(a2, brev, vs);
                if (T) g = cmul(g, coef);
                const cf rb_ = T == 0 ? e8 : T == 1 ? e1 : T == 3 ? e128 : cf{hh ? e128.r : e2.r, hh ? e128.i : e2.i};
                const cf g4_ = T == 2 ? cmul(g, cf{hh ? e4.r : e128.r, hh ? e4.i : e128.i}) : cf{0.f, 0.f};
                asm volatile("" ::: "memory");
#pragma unroll
                for (int r = 0; r < 8; ++r) {
                    *(f32x2*)(wr0 + r * 256 + ((wx ^ r) << 4)) = f32x2{g.r, T ? -g.i : g.i};
                    if (r < 7) g = (T == 2 && r == 3) ? g4_ : cmul(g, rb_);
                }
                asm volatile("" ::: "memory");
                f32x4 ga[4];
#pragma unroll
                for (int kk = 0; kk < 4; ++kk) ga[kk] = *(const f32x4*)(scr + i16 * 256 + (((kk * 4 + g4) ^ i16) << 4));
                if (T == 0) {
#pragma unroll
                    for (int kk = 0; kk < 4; ++kk) fb[kk] = ga[kk];
                } else {
#pragma unroll
                    for (int kk = 0; kk < 4; ++kk) {
                        res[T - 1][0] = __builtin_amdgcn_mfma_f32_16x16x4f32(ga[kk][0], fb[kk][0], res[T - 1][0], 0, 0, 0);
                        res[T - 1][1] = __builtin_amdgcn_mfma_f32_16x16x4f32(ga[kk][1], fb[kk][1], res[T - 1][1], 0, 0, 0);
                        res[T - 1][0] = __builtin_amdgcn_mfma_f32_16x16x4f32(ga[kk][2], fb[kk][2], res[T - 1][0], 0, 0, 0);
                        res[T - 1][1] = __builtin_amdgcn_mfma_f32_16x16x4f32(ga[kk][3], fb[kk][3], res[T - 1][1], 0, 0, 0);
                    }
                }
            }
        }
        float* kd = (float*)(lds + T_KD + rg * KDB);
#pragma unroll
        for (int e = 0; e < 4; ++e) {
            const int i = g4 * 4 + e, j = i16;
            const int id1 = i < 8 ? 256 + (i >> 2) * 64 + (i & 3) + 4 * j : 384 + (i & 3) * 32 + ((i >> 2) & 1) + 2 * j;
            const int id2 = 512 + i * 16 + j;
            kd[(i >> 3) * 128 + (i & 7) + 8 * j] = res[0][0][e] + res[0][1][e];
            kd[id1] = res[1][0][e] + res[1][1][e];
            if (id2 <= 640) kd[id2] = res[2][0][e] + res[2][1][e];
        }
    }
    ST(9)
    role_sync(cnt, 4, lane);
    ST(10)
    char* kp = lds + T_KP + ch * 12288;
    char* uc = lds + T_UC + ch * (8 * UCOPY);
    {
        const float* kd0 = (const float*)(lds + T_KD + (ch * 2) * KDB), * kd1 = (const float*)(lds + T_KD + (ch * 2 + 1) * KDB);
        float kv[8];
        if (wi == 0) {
            const int base = lane < 32 ? 8 * lane : 128 + 4 * lane;
            const f32x4 a0 = *(const f32x4*)(kd0 + base) + *(const f32x4*)(kd1 + base);
            const f32x4 a1 = *(const f32x4*)(kd0 + base + 4) + *(const f32x4*)(kd1 + base + 4);
            const bool ex = lane < 32;
            kv[0] = a0[0]; kv[1] = ex ? a0[1] : a0[0]; kv[2] = ex ? a0[2] : a0[1]; kv[3] = ex ? a0[3] : a0[2];
            kv[4] = ex ? a1[0] : a0[2]; kv[5] = ex ? a1[1] : a0[2]; kv[6] = ex ? a1[2] : a0[3]; kv[7] = ex ? a1[3] : a1[0];
        } else {
            const int base = 256 + 2 * t7;
            const f32x2 a0 = *(const f32x2*)(kd0 + base) + *(const f32x2*)(kd1 + base);
            const float a2 = kd0[base + 2] + kd1[base + 2];
            kv[0] = kv[1] = kv[2] = a0[0]; kv[3] = kv[4] = kv[5] = a0[1]; kv[6] = kv[7] = a2;
        }
        hilo_store(kp, t7, kv);
        {
            const int c8 = 128 + t7, base = 384 + c8;
            const float a0 = kd0[base] + kd1[base], a1 = kd0[base + 1] + kd1[base + 1];
            kv[0] = kv[1] = kv[2] = kv[3] = a0; kv[4] = (c8 & 1) ? a1 : a0; kv[5] = kv[6] = kv[7] = a1;
            hilo_store(kp, c8, kv);
        }
        {
            const int pl = t7 >> 5, rr = t7 & 31;
            *(u32x4*)(kp + pl * 1536 + rr * 16) = u32x4{0, 0, 0, 0};
            *(u32x4*)(kp + 6144 + pl * 1536 + rr * 16) = u32x4{0, 0, 0, 0};
        }
        const unsigned sel = ch ? 0x03020706u : 0x01000504u;
#pragma unroll 1
        for (int it = 0; it < 2; ++it) {
            const int X = t7 + it * 128;
            const char* ul = lds + T_U2 + X * 32 - 32;
            const u32x4 x0 = *(const u32x4*)(ul), x1 = *(const u32x4*)(ul + 16), x2 = *(const u32x4*)(ul + 32), x3 = *(const u32x4*)(ul + 48);
            const unsigned xx[16] = {x0[0], x0[1], x0[2], x0[3], x1[0], x1[1], x1[2], x1[3], x2[0], x2[1], x2[2], x2[3], x3[0], x3[1], x3[2], x3[3]};
            unsigned dd[8];
#pragma unroll
            for (int k = 0; k < 8; ++k) dd[k] = __builtin_amdgcn_perm(xx[15 - 2 * k], xx[14 - 2 * k], sel);
#pragma unroll
            for (int cc = 0; cc < 8; ++cc) {
                const int e = 7 - cc;
                u32x4 v;
#pragma unroll
                for (int k = 0; k < 4; ++k)
                    v[k] = (e & 1) ? __builtin_amdgcn_alignbit(dd[(e + 1) / 2 + k], dd[(e - 1) / 2 + k], 16) : dd[e / 2 + k];
                *(u32x4*)(uc + cc * UCOPY + (X + 3) * 16) = v;
            }
        }
        if (t7 < 24) *(u32x4*)(uc + (t7 & 7) * UCOPY + (t7 >> 3) * 16) = u32x4{0, 0, 0, 0};
    }
    role_sync(cnt, 8, lane);
    ST(11)
    f32x16 y0 = f32x16{}, y1 = f32x16{}, y2 = f32x16{};
    {
        const int tb = -1 + 32 * wi;
        unsigned ub = (unsigned)(uintptr_t)uc + (r32 & 7) * UCOPY + ((r32 >> 3) - hh + 3) * 16 + tb * 32;
        unsigned kb = (unsigned)(uintptr_t)kp + hh * 1536 + (r32 + 32 - 16 * wi - 1) * 16;
        bf16x8 fa[6], fbn[6];
#define CV_ADDR(i, j) ((j) == 0 ? ub + (i) * 32 : (j) == 3 ? ub + (i) * 32 + 2048 :                                   \
                       kb + ((i) < 2 ? 16 : 0) + (((i) & 1) ? 0 : 3072) + ((j) == 2 || (j) == 5 ? 512 : 0) + ((j) >= 4 ? 6144 : 0))
#define CV_STEP(fc, fn, i)                                                                                              \
        _Pragma("unroll") for (int j = 0; j < 6; ++j) {                                                                 \
            fn[j] = lds_rd128(CV_ADDR((i) + 1, j));                                                                     \
            if (j == 0) { y0 = MFMA32(fc[0], fc[1], y0); PIN(y0); } else if (j == 1) { y1 = MFMA32(fc[0], fc[2], y1); PIN(y1); }   \
            else if (j == 2) { y2 = MFMA32(fc[3], fc[1], y2); PIN(y2); } else if (j == 3) { y0 = MFMA32(fc[0], fc[4], y0); PIN(y0); } \
            else if (j == 4) { y1 = MFMA32(fc[0], fc[5], y1); PIN(y1); } else { y2 = MFMA32(fc[3], fc[4], y2); PIN(y2); } \
            SBAR();                                                                                                     \
        }
#pragma unroll
        for (int j = 0; j < 6; ++j) fa[j] = lds_rd128(CV_ADDR(0, j));
#pragma unroll 1
        for (int pr = 0; pr < 16; ++pr) {
            CV_STEP(fa, fbn, 0) CV_STEP(fbn, fa, 1)
            ub += 64; kb -= 16;
        }
    }
#pragma unroll
    for (int r = 0; r < 16; ++r) y1[r] += y2[r];
    ST(12)
    role_sync(cnt, 12, lane);
    {
        char* xb = lds + T_UC + rg * 4096;
        const f32x16 give = wi ? y0 : y1;
#pragma unroll
        for (int g = 0; g < 4; ++g) *(f32x4*)(xb + g * 1024 + l16) = f32x4{give[4 * g], give[4 * g + 1], give[4 * g + 2], give[4 * g + 3]};
    }
    role_sync(cnt, 16, lane);
    {
        const char* xb = lds + T_UC + (rg ^ 1) * 4096;
        const f32x16 keep = wi ? y1 : y0;
        float* yb = (float*)(lds + T_UC + 16384) + ch * (64 * 36) + (wi * 32 + r32) * 36 + 4 * hh;
#pragma unroll
        for (int g = 0; g < 4; ++g) {
            const f32x4 v = *(const f32x4*)(xb + g * 1024 + l16);
            *(f32x4*)(yb + 8 * g) = f32x4{keep[4 * g] + v[0], keep[4 * g + 1] + v[1], keep[4 * g + 2] + v[2], keep[4 * g + 3] + v[3]};
        }
    }
    role_sync(cnt, 20, lane);
    ST(13)
    {
        const unsigned dsk = *(const unsigned*)(lds + T_PRM + 3076);
        const float ds0 = __uint_as_float(dsk << 16), ds1 = __uint_as_float(dsk & 0xffff0000u);
        const float* ya = (const float*)(lds + T_UC + 16384);
#pragma unroll 2
        for (int k = 0; k < 8; ++k) {
            const int l = t8 + 256 * k, yi = (l >> 5) * 36 + (l & 31);
            const unsigned upk = *(const unsigned*)(lds + T_U2 + l * 4);
            const float u0 = __uint_as_float(upk << 16), u1 = __uint_as_float(upk & 0xffff0000u);
            const float o0 = bfround(ya[yi]) + bfround(u0 * ds0);
            const float o1 = bfround(ya[64 * 36 + yi]) + bfround(u1 * ds1);
            *(unsigned*)(out + (size_t)l * 1024 + 512 + d0) = cvtpk(o0, o1);
        }
    }
    ST_END()
}

__global__ __launch_bounds__(512, 2) void mega_kernel(cbf x, cbf ckv, cbf wq, cbf wuk, cbf wuv, cbf wo, cbf u, cbf are, cbf aim, cbf bre,
                                                      cbf bim, cbf cre, cbf cim, cbf logdt, cbf dskip, char* __restrict__ ws,
                                                      unsigned short* __restrict__ out) {
    __shared__ __attribute__((aligned(16))) char lds[163840];
    const int tid = threadIdx.x, lane = tid & 63, w = rfl(tid >> 6), r32 = lane & 31, hh = lane >> 5;
    const int b = blockIdx.x, xcd = b & 7, idx = b >> 3, h = xcd * 4 + (idx & 3), rb = idx >> 2;
    const int bi = rb, bj = 15 - rb, grp = w >> 2, rg = w & 3;
    const unsigned l16 = lane * 16;
    gu64* epoch = (gu64*)(ws + WS_FLAG);
    gu64* acks = (gu64*)(ws + WS_FLAG + 4096);
    gu32* ctr = (gu32*)(ws + WS_FLAG + 8192);
    AT c;
    c.ws = ws; c.h = h; c.bi = bi; c.bj = bj; c.grp = grp; c.rg = rg; c.qrel = (lane & 15) - 4 * (lane >> 4); c.w = w;
    c.lb = (unsigned)(uintptr_t)lds; c.l16 = l16;
    c.ldp = ws + WS_KV + ((size_t)h << 20) + w * 4096;
    c.qh = ws + WS_Q + ((size_t)b << 15);
    c.ldbase = c.lb + (grp ? 0 : 98304) + rg * 4096;
    c.ldoff = 0; c.ldlim = grp ? 98304 : 65536; c.koff = 16384; c.voff = 0; c.taun = 1; c.ldleft = 32 - 2 * bi;
    {
    constexpr unsigned S0 = 0u, S1 = 32768u, S2 = 65536u, S3 = 98304u, S4 = 131072u;
    PJ p;
    p.lb = c.lb;
    {
        const int r8 = lane >> 3, sl8 = lane & 7, r4 = lane >> 4, sl16 = lane & 15;
        p.xoff0 = r8 * 1024 + ((sl8 ^ (r8 >> 1)) << 4);
        p.xoff1 = r8 * 1024 + ((sl8 ^ (4 + (r8 >> 1))) << 4);
        p.woff[0] = r4 * 8192 + ((sl16 ^ ((r4 << 2) | ((2 * w) & 3))) << 4);
        p.woff[1] = r4 * 8192 + ((sl16 ^ ((r4 << 2) | ((2 * w + 1) & 3))) << 4);
    }
    p.xdst = p.lb + w * 4096; p.wdst = p.lb + w * 2048;
    const size_t wrow_off = (size_t)(w * 8) * 8192 + h * 256;
    p.xs = (const char*)ckv + (size_t)((rg * 8 + rb) * 64 + grp * 32) * 1024; p.w1 = (const char*)wuk + wrow_off; p.w2 = (const char*)wuv + wrow_off;
#define XADV() p.xs += 128
#define WADV() { p.w1 += 64 * 8192; p.w2 += 64 * 8192; }
    const int sh = idx * 8 + w;
    const size_t warm_x = (size_t)(sh * 8 + (lane & 7)) * 1024 + ((lane >> 3) & 1) * 128;
    const size_t warm_w = (size_t)(sh >> 1) * 8192 + xcd * 1024 + ((sh & 1) * 4 + (lane & 3)) * 128;
    p.winc = lane < 16 ? 256u : 128u * 8192u;
    p.wp = (lane < 16 ? (const char*)ckv + warm_x : (lane < 20 ? (const char*)wuk : (const char*)wuv) + warm_w) + (size_t)p.winc;
    p.dummy = 0;
    const unsigned vzero = 0;
    u64 ep;
    asm volatile("global_load_dwordx2 %0, %1, %2 sc1" : "=v"(ep) : "v"(vzero), "s"(epoch) : "memory");
    if (b == 0 && w == 0) {
#pragma unroll
        for (int k = 0; k < 3; ++k) if (k * 64 + lane < C_NUM) __hip_atomic_store(ctr + (k * 64 + lane) * 32, 0u, RLX_AGENT);
    }
#pragma unroll
    for (int t = 0; t < 2; ++t) {
#pragma unroll
        for (int q = 0; q < 4; ++q) { pj_x(p, q, t ? S1 : S0); pj_w(p, q, t ? S3 : S2); }
        XADV(); WADV();
    }
    pj_warm(p);
    asm volatile("s_waitcnt vmcnt(9)" ::: "memory");
    PIN(ep);
    ep = ((u64)rfl((unsigned)(ep >> 32)) << 32) | rfl((unsigned)ep);
    if (tid == 0) __hip_atomic_store(acks + b, ack_of(ep, b), RLX_AGENT);
    ST(14)
    const int r16 = lane & 15, g4 = lane >> 4, q4 = r16 >> 2, p4 = lane & 3;
#define WBASE(cc, t) (p.lb + (8 * g4 + 4 * (t) + q4) * 256 + (((2 * (cc) + (p4 >> 1)) ^ ((q4 << 2) | ((g4 & 1) << 1) | (t))) << 4) + (p4 & 1) * 8)
    const int th = w >> 2, dq = w & 3;
    const int tq = w >> 1, dh = w & 1;
#pragma unroll
    for (int s = 0; s < 2; ++s) p.xa[s] = p.lb + th * 16384 + r16 * 128 + (((4 * s + g4) ^ ((r16 >> 1) & 7)) << 4);
#pragma unroll
    for (int i = 0; i < 4; ++i) p.wa[i >> 1][i & 1] = WBASE(2 * dq + (i >> 1), i & 1);
    SYNC_LDS();
    ST(2)
    FW f0, f1;
    bf16x8 xr[8];
    bf16x8 kvp[16];
    {
        f32x4 acck[8][2], accv[8][2];
#pragma unroll
        for (int i = 0; i < 16; ++i) { acck[i >> 1][i & 1] = f32x4{}; accv[i >> 1][i & 1] = f32x4{}; }
#pragma unroll
        for (int j = 0; j < 8; ++j) kvw_read(p, f0, j, 0, S2);
#pragma unroll
        for (int j = 0; j < 4; ++j) xr[j] = x_read(p, j, 0, S0);
#define KV_QUARTER(Q, fc, fn, xb, wb, xn, wn, RD, H1, H2, H3)                                                              \
        _Pragma("unroll") for (int gi = 0; gi < 4; ++gi) {                                                                \
            const int G = 4 * (Q) + gi, tt = G & 7, N = G + 4;                                                            \
            if (N < 16) xr[N & 7] = x_read(p, N & 7, N >> 3, xb); else if (RD) xr[N & 7] = x_read(p, N & 7, 0, xn);       \
            acck[tt][0] = MFMA16(fw(fc, 0), xr[tt], acck[tt][0]); PIN(acck[tt][0]);                                       \
            SBAR();                                                                                                       \
            if ((Q) < 2) kvw_read(p, fn, G, 1, wb); else if ((Q) == 3 && RD) kvw_read(p, fn, 2 * gi, 0, wn);              \
            acck[tt][1] = MFMA16(fw(fc, 1), xr[tt], acck[tt][1]); PIN(acck[tt][1]);                                       \
            SBAR();                                                                                                       \
            if ((Q) == 3 && RD) kvw_read(p, fn, 2 * gi + 1, 0, wn);                                                       \
            accv[tt][0] = MFMA16(xr[tt], fw(fc, 2), accv[tt][0]); PIN(accv[tt][0]);                                       \
            SBAR();                                                                                                       \
            accv[tt][1] = MFMA16(xr[tt], fw(fc, 3), accv[tt][1]); PIN(accv[tt][1]);                                       \
            if (gi == 0) { H1; } if (gi == 2) { H2; } if (gi == 3) { H3; }                                                \
            SBAR();                                                                                                       \
        }
        KV_QUARTER(0, f0, f1, S0, S2, 0u, 0u, true, pj_warm(p), , )
        KV_QUARTER(1, f0, f1, S0, S2, 0u, 0u, true, pj_w(p, 0, S4), pj_w(p, 1, S4), )
        KV_QUARTER(2, f1, f0, S0, S2, 0u, 0u, true, pj_w(p, 2, S4), pj_w(p, 3, S4), WADV())
        SYNC_N(5);
        KV_QUARTER(3, f1, f0, S0, S2, S1, S3, true, pj_x(p, 0, S0), pj_x(p, 1, S0), )
        unsigned wcur = S3, wnext = S4, wld = S2;
#pragma unroll 1
        for (int t = 1; t < 6; ++t) {
            const unsigned xb = (t & 1) << 15, xn = xb ^ S1;
            if (t == 2) p.wp = lane < 16 ? (const char*)x + warm_x : (const char*)wq + warm_w;
            if (t == 2 && b == 0 && w == 0) {
                unsigned spins = 0;
                for (;;) {
                    bool ok = true;
#pragma unroll
                    for (int k = 0; k < 4; ++k) ok &= __hip_atomic_load(acks + k * 64 + lane, RLX_AGENT) == ack_of(ep, k * 64 + lane);
                    if (__all(ok) || ++spins >= SPIN_CAP) break;
                    __builtin_amdgcn_s_sleep(2);
                }
                asm volatile("s_waitcnt vmcnt(0)" ::: "memory");
                if (lane == 0) __hip_atomic_store(epoch, ep + 1, RLX_AGENT);
            }
            KV_QUARTER(0, f0, f1, xb, wcur, 0u, 0u, true, pj_x(p, 2, xn), pj_x(p, 3, xn); XADV(), pj_warm(p))
            KV_QUARTER(1, f0, f1, xb, wcur, 0u, 0u, true, pj_w(p, 0, wld), pj_w(p, 1, wld), )
            KV_QUARTER(2, f1, f0, xb, wcur, 0u, 0u, true, pj_w(p, 2, wld), pj_w(p, 3, wld), WADV())
            SYNC_N(5);
            KV_QUARTER(3, f1, f0, xb, wcur, xn, wnext, true, pj_x(p, 0, xb), pj_x(p, 1, xb), )
            const unsigned tmp = wcur; wcur = wnext; wnext = wld; wld = tmp;
        }
        SBAR();
#define WO_DMA(q) glds1((const char*)wo + (size_t)((b >> 1) * 32 + (w * 2 + (q)) * 2) * 1024 + (b & 1) * 512, (lane >> 5) * 1024 + (lane & 31) * 16, p.lb + S4 + 16384 + (w * 2 + (q)) * 1024)
        p.w1 = (const char*)wq + wrow_off;
        KV_QUARTER(0, f0, f1, S0, S2, 0u, 0u, true, pj_x(p, 2, S1), pj_x(p, 3, S1), )
        p.xs = (const char*)x + (size_t)((grp ? bj : bi) * 128 + rg * 32) * 1024;
        KV_QUARTER(1, f0, f1, S0, S2, 0u, 0u, true, pj_w(p, 0, S4), pj_w(p, 1, S4), WADV())
        KV_QUARTER(2, f1, f0, S0, S2, 0u, 0u, true, WO_DMA(0), WO_DMA(1), )
        SYNC_N(4);
        KV_QUARTER(3, f1, f0, S0, S2, S1, S3, true, pj_x(p, 0, S0), pj_x(p, 1, S0), )
        KV_QUARTER(0, f0, f1, S1, S3, 0u, 0u, true, pj_x(p, 2, S0), pj_x(p, 3, S0), XADV())
        KV_QUARTER(1, f0, f1, S1, S3, 0u, 0u, true, pj_w(p, 0, S2), pj_w(p, 1, S2), WADV())
        KV_QUARTER(2, f1, f0, S1, S3, 0u, 0u, true, pj_w(p, 0, S2 + 16384), pj_w(p, 1, S2 + 16384), WADV())
        SYNC_N(4);
        asm volatile("" :: "v"(p.dummy));
        KV_QUARTER(3, f1, f0, S1, S3, 0u, 0u, false, pj_x(p, 0, S1), pj_x(p, 1, S1), )
        ST(3)
#pragma unroll
        for (int i = 0; i < 8; ++i) p.wa[i >> 1][i & 1] = WBASE(4 * dh + (i >> 1), i & 1);
#pragma unroll
        for (int s = 0; s < 2; ++s) p.xa[s] += tq * 8192 - th * 16384;
#pragma unroll
        for (int j = 0; j < 8; ++j) qw_read(p, f0, j, 0, S4);
#pragma unroll
        for (int j = 0; j < 2; ++j) xr[j] = x_read(p, j, 0, S0);
#pragma unroll
        for (int tb = 0; tb < 4; ++tb)
#pragma unroll
            for (int i = 0; i < 2; ++i) {
                kvp[tb * 4 + i * 2] = pack2(acck[2 * tb + i][0], acck[2 * tb + i][1]);
                kvp[tb * 4 + i * 2 + 1] = pack2(accv[2 * tb][i], accv[2 * tb + 1][i]);
                PIN(kvp[tb * 4 + i * 2]); PIN(kvp[tb * 4 + i * 2 + 1]);
            }
    }
    ST(4)
    {
        f32x4 acc[4][4];
#pragma unroll
        for (int i = 0; i < 16; ++i) acc[i >> 2][i & 3] = f32x4{};
        char* kvt = ws + WS_KV + ((size_t)(h * 32 + rb) << 15);
#define KVST(i) st16(kvt + ((size_t)((i) >> 2) << 18) + (((i) & 1) ? 16384 + (((dq * 2 + (((i) >> 1) & 1)) * 2 + th) << 10)            \
                                                                  : (((th * 2 + (((i) >> 1) & 1)) * 4 + dq) << 10)), l16, kvp[i])
#define KVSIG(g) if (w == 0) atomic_inc1(ws, C_KV + h * 4 + (g), l16, 1u);
#define WO_ST(it) { const int f = w + (it) * 8; const unsigned a = p.lb + S4 + 16384 + (4 * g4 + q4) * 512 + (f >> 2) * 128 + p4 * 32 + (f & 3) * 8;   \
                    st16(ws + WS_WO + ((size_t)(((b & 1) * 16 + f) * 128 + (b >> 1)) << 10), l16, cat8(lds_tr(a), lds_tr(a + 8192))); }
#define U_DMA() glds1((const char*)u + (size_t)((b >> 2) * 32 + w * 4) * 1024 + (b & 3) * 256, (lane >> 4) * 1024 + (lane & 15) * 16, p.lb + S4 + 16384 + w * 1024)
#define U_ST() { const char* us = lds + S4 + 16384 + (tid & 7) * 1024 + (tid >> 3) * 4;                                                  \
                 const u32x4 v = {*(const unsigned*)us, *(const unsigned*)(us + 256), *(const unsigned*)(us + 512), *(const unsigned*)(us + 768)};   \
                 st16(ws + WS_UT + (size_t)((b & 3) * 64) * 8192 + (b >> 2) * 128, (tid >> 3) * 8192 + (tid & 7) * 16, __builtin_bit_cast(bf16x8, v)); }
#define Q_STEP(Q, fc, fn, xb, wb, xn, wn, RD, H0, H1, H2, H3)                                                             \
        _Pragma("unroll") for (int gi = 0; gi < 2; ++gi) {                                                                \
            const int G = 2 * (Q) + gi, tt = G & 3, N = G + 2;                                                            \
            if (N < 8) xr[N & 3] = x_read(p, N & 3, N >> 2, xb); else if (RD) xr[N & 3] = x_read(p, N & 3, 0, xn);        \
            _Pragma("unroll") for (int j = 0; j < 4; ++j) {                                                               \
                if ((Q) < 2) { if (j & 1) qw_read(p, fn, 2 * G + (j >> 1), 1, opaque(wb)); }                              \
                else if ((Q) == 3 && RD) qw_read(p, fn, 4 * gi + j, 0, opaque(wn));                                       \
                acc[tt][j] = MFMA16(fw(fc, j), xr[tt], acc[tt][j]); PIN(acc[tt][j]);                                      \
                if (j == 1) { if (gi == 0) { H0; } else { H2; } } if (j == 3) { if (gi == 0) { H1; } else { H3; } }       \
                SBAR();                                                                                                   \
            }                                                                                                             \
        }
        constexpr unsigned W0 = S4, W1 = S2, W2 = S2 + 16384;
        u64 ep2; unsigned kvc;
        asm volatile("global_load_dwordx2 %0, %1, %2 sc1" : "=v"(ep2) : "v"(vzero), "s"(epoch) : "memory");
        Q_STEP(0, f0, f1, S0, W0, 0u, 0u, true, KVST(0); KVST(1), KVST(2); KVST(3), pj_x(p, 2, S1), pj_x(p, 3, S1); XADV())
        Q_STEP(1, f0, f1, S0, W0, 0u, 0u, true, pj_x(p, 0, S3), , pj_x(p, 1, S3), )
        Q_STEP(2, f1, f0, S0, W0, 0u, 0u, true, pj_x(p, 2, S3), , pj_x(p, 3, S3); XADV(), )
        SYNC_N(4);
        PIN(ep2);
        if (w == 0) {
            unsigned spins = 0;
            while (ep2 != ep + 1 && ++spins < SPIN_CAP) { __builtin_amdgcn_s_sleep(2); ep2 = __hip_atomic_load(epoch, RLX_AGENT); }
        }
        KVSIG(0)
        Q_STEP(3, f1, f0, S0, W0, S1, W1, true, pj_x(p, 0, S0), , pj_x(p, 1, S0), )
        Q_STEP(0, f0, f1, S1, W1, 0u, 0u, true, pj_x(p, 2, S0), , pj_x(p, 3, S0); XADV(), )
        Q_STEP(1, f0, f1, S1, W1, 0u, 0u, true, pj_w(p, 0, W0), , KVST(4), WO_ST(0))
        Q_STEP(2, f1, f0, S1, W1, 0u, 0u, true, pj_w(p, 1, W0); WADV(), , , WO_ST(1))
        SYNC_N(9);
        Q_STEP(3, f1, f0, S1, W1, S3, W2, true, pj_x(p, 0, S1), , pj_x(p, 1, S1), )
        Q_STEP(0, f0, f1, S3, W2, 0u, 0u, true, pj_x(p, 2, S1), , pj_x(p, 3, S1); XADV(), )
        Q_STEP(1, f0, f1, S3, W2, 0u, 0u, true, pj_w(p, 0, W1), , KVST(5), )
        Q_STEP(2, f1, f0, S3, W2, 0u, 0u, true, pj_w(p, 1, W1); WADV(), , , )
        SYNC_N(8);
        Q_STEP(3, f1, f0, S3, W2, S0, W0, true, pj_x(p, 0, S3), , pj_x(p, 1, S3), U_DMA())
        Q_STEP(0, f0, f1, S0, W0, 0u, 0u, true, pj_x(p, 2, S3), , pj_x(p, 3, S3); XADV(), )
        Q_STEP(1, f0, f1, S0, W0, 0u, 0u, true, pj_w(p, 0, W2), , KVST(6), )
        Q_STEP(2, f1, f0, S0, W0, 0u, 0u, true, pj_w(p, 1, W2); WADV(), , , )
        SYNC_N(8);
        Q_STEP(3, f1, f0, S0, W0, S1, W1, true, pj_x(p, 0, S0), , pj_x(p, 1, S0), )
        asm volatile("global_load_dword %0, %1, %2 sc1" : "=v"(kvc) : "v"((unsigned)(C_KV + h * 4) * 128u), "s"(ws + WS_FLAG + 8192) : "memory");
        Q_STEP(0, f0, f1, S1, W1, 0u, 0u, true, pj_x(p, 2, S0), , pj_x(p, 3, S0); XADV(), )
        Q_STEP(1, f0, f1, S1, W1, 0u, 0u, true, pj_w(p, 0, W0), , KVST(7), )
        Q_STEP(2, f1, f0, S1, W1, 0u, 0u, true, pj_w(p, 1, W0); WADV(), , , )
        SYNC_N(5);
        PIN(kvc);
        if (w == 0) {
            if (rfl(kvc) < 8u) wait_ge(ctr + (C_KV + h * 4) * 32, 8u);
        }
        Q_STEP(3, f1, f0, S1, W1, S3, W2, true, pj_x(p, 0, S1), , pj_x(p, 1, S1), )
        Q_STEP(0, f0, f1, S3, W2, 0u, 0u, true, pj_x(p, 2, S1), , pj_x(p, 3, S1), )
        Q_STEP(1, f0, f1, S3, W2, 0u, 0u, true, pj_w(p, 0, W1), , , )
        Q_STEP(2, f1, f0, S3, W2, 0u, 0u, true, pj_w(p, 1, W1), , , )
        SYNC_N(6);
        KVSIG(1)
#define KSITE(q) if (!grp) { at_piece(c, (q) & 1); if ((q) & 1) at_adv(c); }
#define ASITE(q) { at_piece(c, (q) & 1); if ((q) & 1) at_adv(c); }
        Q_STEP(3, f1, f0, S3, W2, S0, W0, true, KSITE(0), , , )
        const int d0 = xcd * 64 + idx * 2;
        const char* s4p[2];
#pragma unroll
        for (int k = 0; k < 2; ++k) {
            const int e = tid + 512 * k, ar = e >> 7;
            const char* bp = ar == 0 ? (const char*)are : ar == 1 ? (const char*)aim : ar == 2 ? (const char*)bre : ar == 3 ? (const char*)bim :
                             ar == 4 ? (const char*)cre : (const char*)cim;
            s4p[k] = e < 768 ? bp + d0 * 256 + (e & 127) * 4 : (e == 768 ? (const char*)logdt : (const char*)dskip) + d0 * 2;
        }
#define S4P(k) asm volatile("global_load_dword %0, %1, off" : "=v"(c.s4[k]) : "v"(s4p[k]) : "memory")
        Q_STEP(0, f0, f1, S0, W0, 0u, 0u, true, KSITE(1), , KVST(8), )
        Q_STEP(1, f0, f1, S0, W0, 0u, 0u, true, KSITE(2), , U_ST(), )
        Q_STEP(2, f1, f0, S0, W0, 0u, 0u, true, KSITE(3), , KVST(9), )
        if (grp) asm volatile("s_waitcnt vmcnt(3)" ::: "memory"); else asm volatile("s_waitcnt vmcnt(11)" ::: "memory");
        SYNC_LDS();
        Q_STEP(3, f1, f0, S0, W0, S1, W1, true, ASITE(0), , , )
        Q_STEP(0, f0, f1, S1, W1, 0u, 0u, true, ASITE(1), , KVST(10), )
        Q_STEP(1, f0, f1, S1, W1, 0u, 0u, true, ASITE(2), , KVST(11), )
        Q_STEP(2, f1, f0, S1, W1, 0u, 0u, true, ASITE(3), S4P(0), , S4P(1))
        SYNC_LDS();
        Q_STEP(3, f1, f0, S1, W1, 0u, 0u, false, , , , )
        ST(5)
        asm volatile("" :: "v"(p.dummy));
#pragma unroll
        for (int tt = 0; tt < 4; ++tt) {
            const int tk = tq * 2 + (tt >> 1);
            char* xq = lds + S1 + tk * 8192 + l16;
#pragma unroll
            for (int i = 0; i < 2; ++i) {
                const int sfr = (tt & 1) * 4 + dh * 2 + i;
                const bf16x8 v = pack2(acc[tt][2 * i] * QSCALE, acc[tt][2 * i + 1] * QSCALE);
                *(bf16x8*)(xq + sfr * 1024) = v;
                if (tq >= 2) *(bf16x8*)(ws + WS_Q + ((size_t)b << 15) + (tk - 4) * 8192 + sfr * 1024 + l16) = v;
            }
        }
        if (grp) asm volatile("s_waitcnt vmcnt(16)" ::: "memory"); else asm volatile("s_waitcnt vmcnt(13)" ::: "memory");
        SYNC_LDS();
        if (w == 0) atomic_inc1(ws, C_WO, l16, 1u);
        KVST(12); KVST(13); KVST(14); KVST(15);
    }
    }
    const int blk1 = grp ? bj : bi;
    c.kvoff = (C_KV + h * 4 + 1) * 128; c.kvneed = 32 - 2 * bi - 8 + 3; c.kvc = 0;
    c.sigid = C_KV + h * 4 + 2; c.sign = 3;
#pragma unroll
    for (int s = 0; s < 8; ++s) c.qf[s >> 2][s & 3] = lds_rd128(c.lb + 32768 + w * 8192 + s * 1024 + l16);
#pragma unroll
    for (int qt = 0; qt < 2; ++qt) {
        c.negm[qt] = f32x4{}; c.mref[qt] = 0.f; c.ls[qt] = f32x4{}; PIN(c.mref[qt]);
#pragma unroll
        for (int i = 0; i < 8; ++i) c.o[i][qt] = f32x4{};
    }
    c.ones = __builtin_bit_cast(bf16x8, u32x4{0x3f803f80u, 0x3f803f80u, 0x3f803f80u, 0x3f803f80u}); PIN(c.ones);
    SB sA, sB;
    {
        const unsigned ks_ = c.lb + 98304 + l16;
#pragma unroll
        for (int i = 0; i < 16; ++i) {
            const bf16x8 kf = lds_rd128(ks_ + kitem(i));
            const int kt = i & 3, ds = i >> 2;
            sA.s[kt][0] = MFMA16(kf, c.qf[0][ds], ds ? sA.s[kt][0] : f32x4{});
            sA.s[kt][1] = MFMA16(kf, c.qf[1][ds], ds ? sA.s[kt][1] : f32x4{});
        }
#pragma unroll
        for (int i = 0; i < NPRE; ++i) c.fr[i] = lds_rd128(ks_ + 16384 + kitem(i));
        at_mask(c, sA, 0, blk1);
        at_check(c, sA, 0.f, true);
    }
    ST(6)
#define PIN_STATE() { PIN(c.mref[0]); PIN(c.mref[1]); }
#pragma unroll 1
    for (int t = 0; t < bi; ++t) {
        at_step<0>(c, sA, sB, 1, 1, blk1, 0);
        at_step<0>(c, sB, sA, 1, 1, blk1, 0);
    }
    PIN_STATE()
    at_step<1>(c, sA, sB, 1, 1 + grp, blk1, 1);
    at_step<2>(c, sB, sA, 1 + grp, 2, bj, 1);
    PIN_STATE()
#pragma unroll 1
    for (int t = 0; t < 7 - bi; ++t) {
        at_step<0>(c, sA, sB, 2, 2, bj, 1);
        at_step<0>(c, sB, sA, 2, 2, bj, 1);
    }
    PIN_STATE()
    unsigned ticket = 0;
    if (w == 0 && c.l16 == 0) ticket = __hip_atomic_fetch_add(ctr + (C_TICK + xcd) * 32, 1u, RLX_AGENT);
    at_x<false>(c, sA, sB);
    at_bar<false>(c);
    tail_part(c, sB, lds, ticket DIAGARGS);
}

extern "C" void kernel_launch(void* const* d_in, const int* in_sizes, int n_in, void* d_out, int out_size, void* d_ws,
                              size_t ws_size, hipStream_t stream) {
    (void)in_sizes; (void)n_in; (void)out_size; (void)ws_size;
    const unsigned short* const* in = (const unsigned short* const*)d_in;
    mega_kernel<<<256, 512, 0, stream>>>(in[0], in[1], in[2], in[3], in[4], in[5], in[6], in[7], in[8], in[9], in[10], in[11],
                                         in[12], in[13], in[14], (char*)d_ws, (unsigned short*)d_out);
}
```

```cpp
#include <hip/hip_runtime.h>
#include <hip/hip_bf16.h>
#include <stdint.h>

typedef __bf16 bf16x8 __attribute__((ext_vector_type(8)));
typedef __bf16 bf16x2 __attribute__((ext_vector_type(2)));
typedef short s16x4 __attribute__((ext_vector_type(4)));
typedef float f32x16 __attribute__((ext_vector_type(16)));
typedef float f32x4 __attribute__((ext_vector_type(4)));
typedef float f32x2 __attribute__((ext_vector_type(2)));
typedef unsigned u32x4 __attribute__((ext_vector_type(4)));
typedef unsigned long long u64;
typedef const unsigned short* __restrict__ cbf;
typedef __attribute__((address_space(1))) unsigned gu32;
typedef __attribute__((address_space(1))) u64 gu64;
#define LAS __attribute__((address_space(3)))
#define DI __device__ __forceinline__
#define MFMA32(a, b, c) __builtin_amdgcn_mfma_f32_32x32x16_bf16((a), (b), (c), 0, 0, 0)
#define MFMA16(a, b, c) __builtin_amdgcn_mfma_f32_16x16x32_bf16((a), (b), (c), 0, 0, 0)
#define RLX_AGENT __ATOMIC_RELAXED, __HIP_MEMORY_SCOPE_AGENT
#define SBAR() __builtin_amdgcn_sched_barrier(0)
#define PIN(x) asm volatile("" : "+v"(x))
#define MX3(a, b, c) __builtin_fmaxf(__builtin_fmaxf((a), (b)), (c))

constexpr int DM = 512;
constexpr float QSCALE = 0.08838834764831845f * 1.4426950408889634f;
#define THRV 40.0f
constexpr float THR = THRV;
constexpr unsigned SPIN_CAP = 1u << 22;
constexpr unsigned CODEWARM = 60000;

constexpr size_t WS_Q = 0;
constexpr size_t WS_UT = 8u << 20;
constexpr size_t WS_KV = 16u << 20;
constexpr size_t WS_CTX = 48u << 20;
constexpr size_t WS_WO = 64u << 20;
constexpr size_t WS_FLAG = 72u << 20;
constexpr int C_KV = 0, C_CTX = 128, C_WO = 144, C_TICK = 145, C_NUM = 153;

DI unsigned cvtpk(float lo, float hi) { f32x2 v = {lo, hi}; bf16x2 b = __builtin_convertvector(v, bf16x2); return __builtin_bit_cast(unsigned, b); }
DI float bf2f(unsigned short b) { return __uint_as_float(((unsigned)b) << 16); }
DI float bfround(float x) { __bf16 b = (__bf16)x; return (float)b; }
DI unsigned rfl(unsigned x) { return __builtin_amdgcn_readfirstlane(x); }
DI bf16x8 pack2(f32x4 x, f32x4 y) {
    u32x4 w = {cvtpk(x[0], x[1]), cvtpk(x[2], x[3]), cvtpk(y[0], y[1]), cvtpk(y[2], y[3])};
    return __builtin_bit_cast(bf16x8, w);
}
DI float gsum(float x) {
    auto s = __builtin_amdgcn_permlane16_swap(__float_as_uint(x), __float_as_uint(x), false, false);
    x = __uint_as_float(s[0]) + __uint_as_float(s[1]);
    auto t = __builtin_amdgcn_permlane32_swap(__float_as_uint(x), __float_as_uint(x), false, false);
    return __uint_as_float(t[0]) + __uint_as_float(t[1]);
}
DI float gmax(float x) {
    auto s = __builtin_amdgcn_permlane16_swap(__float_as_uint(x), __float_as_uint(x), false, false);
    x = fmaxf(__uint_as_float(s[0]), __uint_as_float(s[1]));
    auto t = __builtin_amdgcn_permlane32_swap(__float_as_uint(x), __float_as_uint(x), false, false);
    return fmaxf(__uint_as_float(t[0]), __uint_as_float(t[1]));
}
DI bf16x8 lds_rd128(unsigned addr) { return *(const LAS bf16x8*)(uintptr_t)addr; }
DI s16x4 lds_tr(unsigned addr) { return __builtin_amdgcn_ds_read_tr16_b64_v4i16((LAS s16x4*)(uintptr_t)addr); }
DI bf16x8 cat8(s16x4 lo, s16x4 hi) { return __builtin_bit_cast(bf16x8, __builtin_shufflevector(lo, hi, 0, 1, 2, 3, 4, 5, 6, 7)); }

DI void glds1(const void* sbase, unsigned voff, unsigned m0v) {
    unsigned keep;
    asm volatile("s_mov_b32 %0, m0\n\ts_mov_b32 m0, %3\n\ts_nop 0\n\tglobal_load_lds_dwordx4 %1, %2\n\ts_mov_b32 m0, %0"
                 : "=&s"(keep) : "v"(voff), "s"(sbase), "s"(m0v) : "memory");
}
template <int OFF> DI void glds2m(const void* sbase, unsigned voff, unsigned m0v, int left) {
    unsigned keep;
    asm volatile("s_mov_b32 %0, m0\n\ts_mov_b32 m0, %3\n\ts_cmp_gt_i32 %4, 0\n\ts_cselect_b64 exec, -1, 0\n\t"
                 "global_load_lds_dwordx4 %1, %2 offset:%5 sc1\n\tglobal_load_lds_dwordx4 %1, %2 offset:%6 sc1\n\ts_mov_b64 exec, -1\n\ts_mov_b32 m0, %0"
                 : "=&s"(keep) : "v"(voff), "s"(sbase), "s"(m0v), "s"(left), "i"(OFF), "i"(OFF + 1024) : "memory", "scc");
}
DI void glds4(const void* sbase, unsigned voff, unsigned m0v) {
    unsigned keep;
    asm volatile("s_mov_b32 %0, m0\n\ts_mov_b32 m0, %3\n\ts_nop 0\n\t"
                 "global_load_lds_dwordx4 %1, %2\n\tglobal_load_lds_dwordx4 %1, %2 offset:1024\n\t"
                 "global_load_lds_dwordx4 %1, %2 offset:2048\n\tglobal_load_lds_dwordx4 %1, %2 offset:3072\n\ts_mov_b32 m0, %0"
                 : "=&s"(keep) : "v"(voff), "s"(sbase), "s"(m0v) : "memory");
}
DI void st16(char* ubase, unsigned voff, bf16x8 v) {
    __amdgpu_buffer_rsrc_t r = __builtin_amdgcn_make_buffer_rsrc(ubase, (short)0, 0x7fffffff, 0x00020000);
    __builtin_amdgcn_raw_buffer_store_b128(__builtin_bit_cast(u32x4, v), r, voff, 0, 16);
}
DI f32x4 mfma_c(bf16x8 a, bf16x8 b, const f32x4& cc) {
    f32x4 d;
    asm volatile("v_mfma_f32_16x16x32_bf16 %0, %1, %2, %3" : "=&v"(d) : "v"(a), "v"(b), "v"(cc));
    return d;
}
DI void mfma_acc(f32x4& acc, bf16x8 a, bf16x8 b) { asm volatile("v_mfma_f32_16x16x32_bf16 %0, %1, %2, %0" : "+v"(acc) : "v"(a), "v"(b)); }
#define MFMA_DONE() { SBAR(); asm volatile("s_nop 7"); SBAR(); }
#define SYNC_N(n) asm volatile("s_waitcnt vmcnt(" #n ") lgkmcnt(0)\n\ts_barrier" ::: "memory")
#define SYNC_ALL() SYNC_N(0)
#define SYNC_LDS() asm volatile("s_waitcnt lgkmcnt(0)\n\ts_barrier" ::: "memory")
DI void atomic_inc1(char* ws, unsigned id, unsigned l16, unsigned mask) {
    const unsigned one = 1;
    asm volatile("s_mov_b32 exec_lo, %3\n\ts_mov_b32 exec_hi, 0\n\tglobal_atomic_add %0, %1, %2\n\ts_mov_b64 exec, -1"
                 :: "v"(l16 * 8 + id * 128), "v"(one), "s"(ws + WS_FLAG + 8192), "s"(mask) : "memory");
}
DI void wait_ge(gu32* p, unsigned want) {
    unsigned spins = 0;
    while (rfl(__hip_atomic_load(p, RLX_AGENT)) < want && ++spins < SPIN_CAP) __builtin_amdgcn_s_sleep(2);
}
DI u64 ack_of(u64 e, int wg) { return (e ^ 0x9E3779B97F4A7C15ull) + (u64)(wg + 1) * 0x100000001B3ull; }

#define ST(i)
#define ST_END()

DI unsigned opaque(unsigned x) { asm volatile("" : "+s"(x)); return x; }
struct PJ {
    const char* xs; const char* w1; const char* w2;
    const char* wp; unsigned winc, dummy;
    unsigned lb, xoff0, xoff1, woff[2], xdst, wdst;
    unsigned xa[2], wa[4][2];
};
DI void pj_x(const PJ& c, int q, unsigned xb) { glds1(c.xs + q * 8192, (q & 1) ? c.xoff1 : c.xoff0, c.xdst + xb + q * 1024); }
DI void pj_w(const PJ& c, int q, unsigned wb) { glds1((q < 2 ? c.w1 : c.w2) + (q & 1) * 32768, c.woff[q & 1], c.wdst + wb + (q >> 1) * 16384 + (q & 1) * 1024); }
DI void pj_warm(PJ& c) {
    asm volatile("s_mov_b64 exec, 0xffffff\n\tglobal_load_dword %0, %1, off\n\ts_mov_b64 exec, -1" : "+v"(c.dummy) : "v"(c.wp) : "memory");
    c.wp += c.winc;
}
struct FW { s16x4 h[4][2]; };
DI bf16x8 fw(const FW& f, int i) { return cat8(f.h[i][0], f.h[i][1]); }
DI void kvw_read(const PJ& c, FW& f, int j, int s, unsigned wb) { f.h[j >> 1][j & 1] = lds_tr(c.wa[(j >> 1) & 1][j & 1] + wb + (j >> 2) * 16384 + s * 8192); }
DI void qw_read(const PJ& c, FW& f, int j, int s, unsigned wb) { f.h[j >> 1][j & 1] = lds_tr(c.wa[j >> 1][j & 1] + wb + s * 8192); }
DI bf16x8 x_read(const PJ& c, int tt, int s, unsigned xb) { return lds_rd128(c.xa[s] + xb + tt * 2048); }

#define NPREV 6
constexpr int NPRE = NPREV;
struct SB { f32x4 s[4][2]; };
struct AT {
    char* ws; const char* ldp; const char* qh;
    unsigned sign, sigid;
    unsigned kvoff; int kvneed; unsigned kvc;
    int h, bi, bj, grp, rg, qrel, w;
    unsigned lb, l16;
    unsigned ldoff, ldlim, ldbase;
    unsigned koff, voff;
    int taun, ldleft;
    bf16x8 qf[2][4], fr[8];
    unsigned pw[16];
    f32x4 o[8][2], negm[2];
    f32x4 ls[2];
    bf16x8 ones;
    unsigned s4[2];
    float mref[2];
};
DI void at_piece(AT& c, int q) {
    const unsigned m0v = c.ldbase + c.ldoff;
    if (q == 0) glds2m<0>(c.ldp, c.l16, m0v, c.ldleft); else glds2m<2048>(c.ldp, c.l16, m0v, c.ldleft);
}
DI void at_adv(AT& c, int on = 1) { c.ldp += on * 32768; c.ldoff += on * 16384; if (c.ldoff >= c.ldlim) c.ldoff = 0; c.ldleft -= on; }
DI void at_load_tile(AT& c) { glds4(c.ldp, c.l16, c.ldbase + c.ldoff); at_adv(c); }
DI unsigned kitem(int j) { return ((j & 3) * 4 + (j >> 2)) * 1024; }
DI unsigned vitem(int j) { return ((j & 7) * 2 + (j >> 3)) * 1024; }
#define SEL(S, e) (S).s[((e) >> 4) * 2 + (((e) >> 2) & 1)][((e) >> 3) & 1][(e) & 3]

DI void at_mask(AT& c, SB& s, int tau, int blk) {
    const int row0 = blk * 128 + c.rg * 32;
    if (tau * 64 + 63 > row0) {
        MFMA_DONE()
        const float dq = (float)(row0 + c.qrel - tau * 64);
#pragma unroll
        for (int kt = 0; kt < 4; ++kt)
#pragma unroll
            for (int qt = 0; qt < 2; ++qt)
#pragma unroll
                for (int e = 0; e < 4; ++e)
                    s.s[kt][qt][e] = __builtin_fmaf(__builtin_fminf(dq - (float)(kt * 16 + e - qt * 16), 0.f), 1e30f, s.s[kt][qt][e]);
    }
#pragma unroll
    for (int i = 0; i < 8; ++i) PIN(s.s[i >> 1][i & 1]);
}
DI void at_check(AT& c, SB& s, float ma, bool first) {
    if (__builtin_expect(first || __any(ma > THR), 0)) {
        MFMA_DONE()
#pragma unroll
        for (int qt = 0; qt < 2; ++qt) {
            float mx = MX3(s.s[0][qt][0], s.s[0][qt][1], s.s[0][qt][2]);
            mx = fmaxf(mx, s.s[0][qt][3]);
#pragma unroll
            for (int kt = 1; kt < 4; ++kt) { mx = MX3(mx, s.s[kt][qt][0], s.s[kt][qt][1]); mx = MX3(mx, s.s[kt][qt][2], s.s[kt][qt][3]); }
            mx = gmax(mx);
            const float dl = first ? mx : __builtin_fmaxf(mx, 0.f);
            c.mref[qt] += dl; PIN(c.mref[qt]);

#pragma unroll
            for (int kt = 0; kt < 4; ++kt) s.s[kt][qt] -= dl;
            c.negm[qt] = f32x4{-c.mref[qt], -c.mref[qt], -c.mref[qt], -c.mref[qt]};
            PIN(c.negm[qt]);
            if (!first) {
                const float fs = __builtin_amdgcn_exp2f(-dl);
                c.ls[qt] *= fs;
#pragma unroll
                for (int d = 0; d < 8; ++d) c.o[d][qt] *= fs;
            }
        }
        asm volatile("s_nop 1");
    }
#pragma unroll
    for (int i = 0; i < 8; ++i) PIN(s.s[i >> 1][i & 1]);
}
template <bool QK> DI void at_x(AT& c, SB& sc, SB& sn) {
    const unsigned ks_ = c.lb + 98304 + c.koff + c.l16, vs_ = c.lb + c.voff + c.l16;
    float e0 = 0.f, e1 = 0.f;
#pragma unroll
    for (int i = 0; i < 16; ++i) {
        const int n = i + NPRE, kt = i & 3, ds = i >> 2;
        if (n < 16) { if (QK) c.fr[n & 7] = lds_rd128(ks_ + kitem(n)); }
        else c.fr[n & 7] = lds_rd128(vs_ + vitem(n - 16));
        if (QK) { if (ds) mfma_acc(sn.s[kt][0], c.fr[i & 7], c.qf[0][ds]); else sn.s[kt][0] = mfma_c(c.fr[i & 7], c.qf[0][0], c.negm[0]); }
        if (i > 0) { c.pw[i - 1] = cvtpk(e0, e1); PIN(c.pw[i - 1]); }
        e0 = __builtin_amdgcn_exp2f(SEL(sc, 2 * i)); PIN(e0);
        SBAR();
        if (QK) { if (ds) mfma_acc(sn.s[kt][1], c.fr[i & 7], c.qf[1][ds]); else sn.s[kt][1] = mfma_c(c.fr[i & 7], c.qf[1][0], c.negm[1]); }
        e1 = __builtin_amdgcn_exp2f(SEL(sc, 2 * i + 1)); PIN(e1);
        SBAR();
    }
    c.pw[15] = cvtpk(e0, e1); PIN(c.pw[15]);
}
template <bool POLL> DI void at_bar(AT& c) {
    if (POLL) asm volatile("s_waitcnt vmcnt(0)\n\ts_barrier" ::: "memory");
    else asm volatile("s_waitcnt vmcnt(0)\n\ts_barrier" ::: "memory");
    if (c.sign) {
        if (c.w == 0) atomic_inc1(c.ws, c.sigid, c.l16, c.sign);
        c.sign = 0;
    }
    PIN(c.kvc);
    if (!POLL) return;
    const bool need = c.ldleft - 2 * c.grp <= c.kvneed && c.kvneed > 3;
    if (need && c.w == 0) {
        if (rfl(c.kvc) < 8u) wait_ge((gu32*)(c.ws + WS_FLAG + 8192 + c.kvoff), 8u);
    }
    c.kvoff += need ? 128 : 0; c.kvneed -= need ? 8 : 0;
    asm volatile("s_cmp_eq_u32 %3, 0\n\ts_cbranch_scc0 .Lskip%=\n\tglobal_load_dword %0, %1, %2 sc1\n.Lskip%=:"
                 : "+v"(c.kvc) : "v"(c.kvoff), "s"(c.ws + WS_FLAG + 8192), "s"(c.w) : "memory", "scc");
}
#define PFRAG(u, qt) __builtin_bit_cast(bf16x8, u32x4{c.pw[((u) * 2 + (qt)) * 4], c.pw[((u) * 2 + (qt)) * 4 + 1], c.pw[((u) * 2 + (qt)) * 4 + 2], c.pw[((u) * 2 + (qt)) * 4 + 3]})
template <bool NEXT, bool QLD> DI float at_y(AT& c, SB& sn, int inc, int blkn, int two) {
    if (NEXT) at_bar<true>(c);
    if (QLD) {
#pragma unroll
        for (int s = 0; s < 8; ++s) c.qf[s >> 2][s & 3] = *(const bf16x8*)(c.qh + c.rg * 8192 + s * 1024 + c.l16);
    }
    if (NEXT) at_mask(c, sn, c.taun, blkn);
    const unsigned vs_ = c.lb + c.voff + c.l16;
    c.koff = (c.koff + inc * 16384) & 65535;
    const unsigned ks_ = c.lb + 98304 + c.koff + c.l16;
    float ma = -__builtin_inff();
#pragma unroll
    for (int i = 0; i < 16; ++i) {
        const int n = i + NPRE, dt = i & 7, u = i >> 3, lq = (i >> 2) & 1;
        if (n < 16) c.fr[n & 7] = lds_rd128(vs_ + vitem(n));
        else if (NEXT) c.fr[n & 7] = lds_rd128(ks_ + kitem(n - 16));
        mfma_acc(c.o[dt][0], c.fr[i & 7], PFRAG(u, 0));
        if ((i & 3) == 3) mfma_acc(c.ls[lq], c.ones, PFRAG(u, lq));
        SBAR();
        mfma_acc(c.o[dt][1], c.fr[i & 7], PFRAG(u, 1));
        if (NEXT) { ma = MX3(ma, SEL(sn, 2 * i), SEL(sn, 2 * i + 1)); PIN(ma); }
#define DPOS 1
        if ((i & 3) == DPOS) {
            const int q = i >> 2;
            if (NEXT && !QLD && (q < 2 || two)) { at_piece(c, q & 1); if (q & 1) at_adv(c); }
        }
        SBAR();
    }
    return ma;
}
template <int KIND> DI void at_step(AT& c, SB& sc, SB& sn, int incv, int inck, int blkn, int two) {
    at_x<true>(c, sc, sn);
    const float ma = at_y<true, KIND == 1>(c, sn, inck, blkn, two);
    c.voff += incv * 16384; if (c.voff >= 98304) c.voff -= 98304;
    c.taun += inck;
    if constexpr (KIND == 2) {
        if (!c.grp) {
            MFMA_DONE()
#pragma unroll
            for (int qt = 0; qt < 2; ++qt) {
                const float inv = 1.0f / c.ls[qt][0];
                char* dst = c.ws + WS_CTX + ((size_t)((c.bi * 8 + c.rg * 2 + qt) * 128 + c.h * 4) << 10);
#pragma unroll
                for (int sp = 0; sp < 4; ++sp) st16(dst + sp * 1024, c.l16, pack2(c.o[2 * sp][qt] * inv, c.o[2 * sp + 1][qt] * inv));
#pragma unroll
                for (int k = 0; k < 8; ++k) c.o[k][qt] = f32x4{};
                c.mref[qt] = 0.f; c.ls[qt] = f32x4{}; PIN(c.mref[qt]);
            }
        }
        PIN(c.mref[0]); PIN(c.mref[1]);
        c.sigid = C_CTX + c.bi; c.sign = 1;
        at_check(c, sn, ma, !c.grp);
    } else {
        at_check(c, sn, ma, false);
    }
    if constexpr (KIND == 1) {
#pragma unroll
        for (int s = 0; s < 8; ++s) PIN(c.qf[s >> 2][s & 3]);
#pragma unroll
        for (int q = 0; q < 4; ++q) { at_piece(c, q & 1); if (q & 1) at_adv(c); }
        if (!c.grp) {
            c.negm[0] = f32x4{}; c.negm[1] = f32x4{}; PIN(c.negm[0]); PIN(c.negm[1]); asm volatile("s_nop 1");
        }
    }
}

struct cf { float r, i; };
DI cf cmul(cf a, cf b) { return cf{a.r * b.r - a.i * b.i, a.r * b.i + a.i * b.r}; }
DI cf cexp_scaled(float a2, float brev, float v) {
    const float e = __builtin_amdgcn_exp2f(a2 * v);
    const float ph = __builtin_amdgcn_fractf(brev * v);
    return cf{e * __builtin_amdgcn_cosf(ph), e * __builtin_amdgcn_sinf(ph)};
}
constexpr int GROW = 528;
constexpr int UCOPY = 4160;
constexpr int T_U2 = 64;
constexpr int T_PRM = 8448;
constexpr int T_RED = 12032;
constexpr int T_UC = T_RED + 49152;
constexpr int T_KP = T_UC + 2 * 8 * UCOPY;
constexpr int T_KD = T_KP + 2 * 12288;
constexpr int KDB = 2592;
constexpr int T_CNT = T_KD + 4 * KDB;
static_assert(T_CNT + 16 <= 163840 && T_CNT >= T_RED + 8 * 9216, "lds");
DI void role_sync(LAS unsigned* cnt, unsigned target, int lane) {
    asm volatile("s_waitcnt lgkmcnt(0)" ::: "memory");
    if (lane == 0) __hip_atomic_fetch_add(cnt, 1u, __ATOMIC_RELAXED, __HIP_MEMORY_SCOPE_WORKGROUP);
    while (__hip_atomic_load(cnt, __ATOMIC_RELAXED, __HIP_MEMORY_SCOPE_WORKGROUP) < target) __builtin_amdgcn_s_sleep(1);
    asm volatile("" ::: "memory");
}
DI void hilo_store(char* kp, int c8, const float (&kv)[8]) {
    unsigned hi[4], lo[4];
#pragma unroll
    for (int e = 0; e < 4; ++e) {
        hi[e] = cvtpk(kv[2 * e], kv[2 * e + 1]);
        lo[e] = cvtpk(kv[2 * e] - __uint_as_float(hi[e] << 16), kv[2 * e + 1] - __uint_as_float(hi[e] & 0xffff0000u));
    }
    *(u32x4*)(kp + (c8 + 32) * 16) = u32x4{hi[0], hi[1], hi[2], hi[3]};
    *(u32x4*)(kp + 4608 + (c8 + 32) * 16) = u32x4{lo[0], lo[1], lo[2], lo[3]};
}
#define ODV 4
constexpr int OD = ODV;

#define DIAGARGS
#define DIAGPARAMS
DI void tail_part(AT& c, SB& sdummy, char* lds, unsigned ticket DIAGPARAMS) {
    unsigned lane_;
    asm volatile("v_mbcnt_lo_u32_b32 %0, -1, 0\n\tv_mbcnt_hi_u32_b32 %0, -1, %0" : "=v"(lane_));
    typedef const void* const __attribute__((address_space(4)))* kargs_t;
    kargs_t ka = (kargs_t)__builtin_amdgcn_kernarg_segment_ptr();
    asm volatile("" : "+s"(ka));
    unsigned short* __restrict__ out = (unsigned short*)ka[16];
    char* ws = c.ws;
    const int lane = lane_, w = c.w, tid = w * 64 + lane, r32 = lane & 31, hh = lane >> 5, grp = c.grp, rg = c.rg, h = c.h, bj = c.bj;
    const unsigned l16 = lane * 16;
    const int b = blockIdx.x, xcd = b & 7, idx = b >> 3;
    gu32* ctr = (gu32*)(ws + WS_FLAG + 8192);
    const int d0 = xcd * 64 + idx * 2;
    unsigned rdy;
    ticket = rfl(ticket);
    const int mb0 = (ticket < 16 ? 0 : 16) + xcd * 2 + ((ticket >> 3) & 1);
    asm volatile("global_load_dword %0, %1, %2 sc1" : "=v"(rdy) : "v"((unsigned)((lane & 1) ? C_CTX + (mb0 >> 1) : C_WO) * 128u), "s"(ws + WS_FLAG + 8192) : "memory");
    SBAR();
    at_y<false, false>(c, sdummy, 0, bj, 0);
    MFMA_DONE()
    ST(7)
    LAS unsigned* cnts = (LAS unsigned*)(uintptr_t)(c.lb + T_CNT);
    {
        const float l0 = c.ls[0][0], l1 = c.ls[1][0];
        SYNC_LDS();
        char* mine = lds + T_RED + w * 9216;
#pragma unroll
        for (int d = 0; d < 4; ++d)
#pragma unroll
            for (int qt = 0; qt < 2; ++qt) {
                const f32x4 oa = c.o[d][qt], ob = c.o[4 + d][qt];
                *(f32x4*)(mine + (d * 2 + qt) * 1024 + l16) = grp ? oa : ob;
            }
        *(f32x4*)(mine + 8192 + l16) = f32x4{c.mref[0], l0, c.mref[1], l1};
        PIN(c.s4[0]); PIN(c.s4[1]);
        *(unsigned*)(lds + T_PRM + tid * 4) = c.s4[0];
        if (tid < 258) *(unsigned*)(lds + T_PRM + 2048 + tid * 4) = c.s4[1];
        asm volatile("s_waitcnt vmcnt(0)" : "+v"(rdy) :: "memory");
        if (w == 0) {
            if (rfl(rdy) < 256u) wait_ge(ctr + C_WO * 32, 256u);
            const bool ok = __builtin_amdgcn_readlane(rdy, 1) >= 32u;
            if (lane == 0) { cnts[0] = 0; cnts[1] = 0; cnts[2] = ticket | (ok ? 256u : 0u); cnts[3] = 0; }
            if (lane < 16) *(unsigned*)(lds + lane * 4) = 0u;
        }
        SYNC_LDS();
        const __amdgpu_buffer_rsrc_t ru = __builtin_amdgcn_make_buffer_rsrc(ws + WS_UT + (size_t)(d0 >> 1) * 8192, (short)0, 8192, 0x00020000);
        const u32x4 uv = __builtin_amdgcn_raw_buffer_load_b128(ru, tid * 16, 0, 16);
        const char* oth = lds + T_RED + (w ^ 4) * 9216;
        const f32x4 ml = *(const f32x4*)(oth + 8192 + l16);
#pragma unroll
        for (int qt = 0; qt < 2; ++qt) {
            const float mo = ml[2 * qt], lo = ml[2 * qt + 1], mm = c.mref[qt], lm = qt ? l1 : l0;
            const float m = fmaxf(mm, mo);
            const float fa = __builtin_amdgcn_exp2f(mm - m), fb = __builtin_amdgcn_exp2f(mo - m);
            const float inv = 1.0f / (lm * fa + lo * fb);
            const float ca = fa * inv, cb = fb * inv;
            char* dst = ws + WS_CTX + ((size_t)((bj * 8 + rg * 2 + qt) * 128 + h * 4 + (grp ? 2 : 0)) << 10);
            f32x4 r[4];
#pragma unroll
            for (int d = 0; d < 4; ++d) {
                const f32x4 oa = c.o[d][qt], ob = c.o[4 + d][qt];
                r[d] = (grp ? ob : oa) * ca + *(const f32x4*)(oth + (d * 2 + qt) * 1024 + l16) * cb;
            }
            st16(dst, l16, pack2(r[0], r[1])); st16(dst + 1024, l16, pack2(r[2], r[3]));
        }
        *(u32x4*)(lds + T_U2 + tid * 16) = uv;
    }
    const unsigned tk = rfl(cnts[2]);
    SYNC_LDS();
    ST(8)
#define CTX_SIGNAL() { asm volatile("s_waitcnt vmcnt(0)" ::: "memory");                                                          \
        if (lane == 0 && __hip_atomic_fetch_add(cnts + 3, 1u, __ATOMIC_RELAXED, __HIP_MEMORY_SCOPE_WORKGROUP) == 7u)             \
            __hip_atomic_fetch_add(ctr + (C_CTX + bj) * 32, 1u, RLX_AGENT); }
    if (!grp) {
        const int mb = ((tk & 255) < 16 ? 0 : 16) + xcd * 2 + ((tk >> 3) & 1), nb = tk & 7;
        const bool early = tk >= 256;
        if (!early) {
            CTX_SIGNAL()
            if (w == 0) wait_ge(ctr + (C_CTX + (mb >> 1)) * 32, 32u);
            role_sync(cnts, 4, lane);
        }
        __builtin_amdgcn_fence(__ATOMIC_ACQUIRE, "wavefront");
        ST(9)
        const __amdgpu_buffer_rsrc_t ra = __builtin_amdgcn_make_buffer_rsrc(ws + WS_CTX + ((size_t)((mb * 4) * 128 + w * 32) << 10), (short)0, 0x7fffffff, 0x00020000);
        const __amdgpu_buffer_rsrc_t rb_ = __builtin_amdgcn_make_buffer_rsrc(ws + WS_WO + ((size_t)((nb * 4) * 128 + w * 32) << 10), (short)0, 0x7fffffff, 0x00020000);
#define LDA(off) __builtin_bit_cast(bf16x8, __builtin_amdgcn_raw_buffer_load_b128(ra, l16, (off), 16))
#define LDB(off) __builtin_bit_cast(bf16x8, __builtin_amdgcn_raw_buffer_load_b128(rb_, l16, (off), 16))
        f32x4 acc[4][4];
#pragma unroll
        for (int i = 0; i < 16; ++i) acc[i >> 2][i & 3] = f32x4{};
        bf16x8 rga[OD][4], rgb[OD][4];
#pragma unroll
        for (int k = 0; k < OD; ++k)
#pragma unroll
            for (int i = 0; i < 4; ++i) { rga[k][i] = LDA(i * 131072 + k * 1024); rgb[k][i] = LDB(i * 131072 + k * 1024); }
        SBAR();
        if (early) {
            asm volatile("s_waitcnt vmcnt(%0)" :: "i"(8 * OD) : "memory");
            if (lane == 0 && __hip_atomic_fetch_add(cnts + 3, 1u, __ATOMIC_RELAXED, __HIP_MEMORY_SCOPE_WORKGROUP) == 7u)
                __hip_atomic_fetch_add(ctr + (C_CTX + bj) * 32, 1u, RLX_AGENT);
        }
        SBAR();
#pragma unroll 1
        for (int k0 = 0; k0 < 32 - OD; k0 += OD) {
#pragma unroll
            for (int k = 0; k < OD; ++k) {
#pragma unroll
                for (int i = 0; i < 16; ++i) acc[i >> 2][i & 3] = MFMA16(rgb[k][i & 3], rga[k][i >> 2], acc[i >> 2][i & 3]);
                SBAR();
                const int kn = (k0 + OD + k) * 1024;
#pragma unroll
                for (int i = 0; i < 4; ++i) { rga[k][i] = LDA(i * 131072 + kn); rgb[k][i] = LDB(i * 131072 + kn); }
                SBAR();
            }
        }
#pragma unroll
        for (int k = 0; k < OD; ++k)
#pragma unroll
            for (int i = 0; i < 16; ++i) acc[i >> 2][i & 3] = MFMA16(rgb[k][i & 3], rga[k][i >> 2], acc[i >> 2][i & 3]);
        ST(10)
#pragma unroll
        for (int i = 0; i < 4; ++i) {
            if (i != w) {
                const int slot = w < i ? w : w - 1;
#pragma unroll
                for (int t = 0; t < 4; ++t) *(f32x4*)(lds + T_RED + (i * 3 + slot) * 4096 + t * 1024 + l16) = acc[i][t];
            }
        }
        role_sync(cnts, early ? 4 : 8, lane);
        unsigned pk[8];
#pragma unroll
        for (int t = 0; t < 4; ++t) {
            f32x4 sm = w == 0 ? acc[0][t] : (w == 1 ? acc[1][t] : (w == 2 ? acc[2][t] : acc[3][t]));
#pragma unroll
            for (int sl = 0; sl < 3; ++sl) sm += *(const f32x4*)(lds + T_RED + (w * 3 + sl) * 4096 + t * 1024 + l16);
            pk[2 * t] = cvtpk(sm[0], sm[1]); pk[2 * t + 1] = cvtpk(sm[2], sm[3]);
        }
        unsigned short* orow = out + (size_t)(mb * 64 + w * 16 + (lane & 15)) * 1024 + nb * 64 + (lane >> 4) * 16;
        *(u32x4*)(orow) = u32x4{pk[0], pk[1], pk[2], pk[3]};
        *(u32x4*)(orow + 8) = u32x4{pk[4], pk[5], pk[6], pk[7]};
        ST_END()
        return;
    }
    const int t8 = tid - 256, ch = rg >> 1, wi = rg & 1, t7 = t8 & 127;
    LAS unsigned* cnt = cnts + 1;
    CTX_SIGNAL()
    {
        const unsigned short* pl = (const unsigned short*)(lds + T_PRM);
        const float dt = bfround(expf(bf2f(pl[1536 + ch])));
        const int i16 = lane & 15, g4 = lane >> 4;
        char* scr = lds + T_UC + rg * 1024;
        {
#define PZ(i) bf2f(pl[(i) * 256 + ch * 128 + wi * 64 + lane])
            const cf coef = cmul(cf{PZ(4), PZ(5)}, cf{dt * PZ(2), dt * PZ(3)});
            const float mx = fmaxf(fabsf(coef.r), fabsf(coef.i)), sr = mx > 0.f ? coef.r / mx : 0.f, si = mx > 0.f ? coef.i / mx : 0.f;
            const float lc = log2f(mx) + 0.5f * log2f(sr * sr + si * si);
            *(f32x4*)(scr + l16) = f32x4{dt * PZ(0) * 1.4426950408889634f, dt * PZ(1) * 0.15915494309189535f, lc,
                                         atan2f(coef.i, coef.r) * 0.15915494309189535f};
        }
        asm volatile("" ::: "memory");
        const float vv[4] = {8.f * i16, (float)((i16 >> 3) * 128 + (i16 & 7)),
                             (float)(i16 < 8 ? 256 + (i16 >> 2) * 128 + (i16 & 3) * 2 : 512 + ((i16 >> 2) & 1) * 4 + (i16 & 3) * 128),
                             (float)(1024 + 128 * (i16 < 8 ? i16 : 8))};
        f32x4 res[3][2];
#pragma unroll
        for (int T = 0; T < 3; ++T) { res[T][0] = f32x4{}; res[T][1] = f32x4{}; }
#pragma unroll 2
        for (int q = 0; q < 16; ++q) {
            const f32x4 m = *(const f32x4*)(scr + (q * 4 + g4) * 16);
            const float fe = __builtin_amdgcn_exp2f(m[0] * vv[0]), fp = __builtin_amdgcn_fractf(m[1] * vv[0]);
            const float fr = fe * __builtin_amdgcn_cosf(fp), fi = -fe * __builtin_amdgcn_sinf(fp);
#pragma unroll
            for (int T = 1; T < 4; ++T) {
                const float ge = __builtin_amdgcn_exp2f(__builtin_fmaf(m[0], vv[T], m[2]));
                const float gp = __builtin_amdgcn_fractf(__builtin_amdgcn_fractf(m[1] * vv[T]) + m[3]);
                res[T - 1][0] = __builtin_amdgcn_mfma_f32_16x16x4f32(ge * __builtin_amdgcn_cosf(gp), fr, res[T - 1][0], 0, 0, 0);
                res[T - 1][1] = __builtin_amdgcn_mfma_f32_16x16x4f32(ge * __builtin_amdgcn_sinf(gp), fi, res[T - 1][1], 0, 0, 0);
            }
        }
        float* kd = (float*)(lds + T_KD + rg * KDB);
#pragma unroll
        for (int e = 0; e < 4; ++e) {
            const int i = g4 * 4 + e, j = i16;
            const int id1 = i < 8 ? 256 + (i >> 2) * 64 + (i & 3) + 4 * j : 384 + (i & 3) * 32 + ((i >> 2) & 1) + 2 * j;
            const int id2 = 512 + i * 16 + j;
            kd[(i >> 3) * 128 + (i & 7) + 8 * j] = res[0][0][e] + res[0][1][e];
            kd[id1] = res[1][0][e] + res[1][1][e];
            if (id2 <= 640) kd[id2] = res[2][0][e] + res[2][1][e];
        }
    }
    ST(9)
    role_sync(cnt, 4, lane);
    ST(10)
    char* kp = lds + T_KP + ch * 12288;
    char* uc = lds + T_UC + ch * (8 * UCOPY);
    {
        const float* kd0 = (const float*)(lds + T_KD + (ch * 2) * KDB), * kd1 = (const float*)(lds + T_KD + (ch * 2 + 1) * KDB);
        float kv[8];
        if (wi == 0) {
            const int base = lane < 32 ? 8 * lane : 128 + 4 * lane;
            const f32x4 a0 = *(const f32x4*)(kd0 + base) + *(const f32x4*)(kd1 + base);
            const f32x4 a1 = *(const f32x4*)(kd0 + base + 4) + *(const f32x4*)(kd1 + base + 4);
            const bool ex = lane < 32;
            kv[0] = a0[0]; kv[1] = ex ? a0[1] : a0[0]; kv[2] = ex ? a0[2] : a0[1]; kv[3] = ex ? a0[3] : a0[2];
            kv[4] = ex ? a1[0] : a0[2]; kv[5] = ex ? a1[1] : a0[2]; kv[6] = ex ? a1[2] : a0[3]; kv[7] = ex ? a1[3] : a1[0];
        } else {
            const int base = 256 + 2 * t7;
            const f32x2 a0 = *(const f32x2*)(kd0 + base) + *(const f32x2*)(kd1 + base);
            const float a2 = kd0[base + 2] + kd1[base + 2];
            kv[0] = kv[1] = kv[2] = a0[0]; kv[3] = kv[4] = kv[5] = a0[1]; kv[6] = kv[7] = a2;
        }
        hilo_store(kp, t7, kv);
        {
            const int c8 = 128 + t7, base = 384 + c8;
            const float a0 = kd0[base] + kd1[base], a1 = kd0[base + 1] + kd1[base + 1];
            kv[0] = kv[1] = kv[2] = kv[3] = a0; kv[4] = (c8 & 1) ? a1 : a0; kv[5] = kv[6] = kv[7] = a1;
            hilo_store(kp, c8, kv);
        }
        if (t7 < 64) *(u32x4*)(kp + (t7 >> 5) * 4608 + (t7 & 31) * 16) = u32x4{0, 0, 0, 0};
        const unsigned sel = ch ? 0x03020706u : 0x01000504u;
#pragma unroll 1
        for (int it = 0; it < 2; ++it) {
            const int X = t7 + it * 128;
            const char* ul = lds + T_U2 + X * 32 - 32;
            const u32x4 x0 = *(const u32x4*)(ul), x1 = *(const u32x4*)(ul + 16), x2 = *(const u32x4*)(ul + 32), x3 = *(const u32x4*)(ul + 48);
            const unsigned xx[16] = {x0[0], x0[1], x0[2], x0[3], x1[0], x1[1], x1[2], x1[3], x2[0], x2[1], x2[2], x2[3], x3[0], x3[1], x3[2], x3[3]};
            unsigned dd[8];
#pragma unroll
            for (int k = 0; k < 8; ++k) dd[k] = __builtin_amdgcn_perm(xx[15 - 2 * k], xx[14 - 2 * k], sel);
#pragma unroll
            for (int cc = 0; cc < 8; ++cc) {
                const int e = 7 - cc;
                u32x4 v;
#pragma unroll
                for (int k = 0; k < 4; ++k)
                    v[k] = (e & 1) ? __builtin_amdgcn_alignbit(dd[(e + 1) / 2 + k], dd[(e - 1) / 2 + k], 16) : dd[e / 2 + k];
                *(u32x4*)(uc + cc * UCOPY + (X + 3) * 16) = v;
            }
        }
        if (t7 < 24) *(u32x4*)(uc + (t7 & 7) * UCOPY + (t7 >> 3) * 16) = u32x4{0, 0, 0, 0};
    }
    role_sync(cnt, 8, lane);
    ST(11)
    f32x4 y[8];
#pragma unroll
    for (int j = 0; j < 8; ++j) y[j] = f32x4{};
    {
        const int r16 = lane & 15, g4 = lane >> 4;
        unsigned ub = (unsigned)(uintptr_t)uc + (r16 & 7) * UCOPY + ((r16 >> 3) - g4 + 5) * 16 + wi * 256;
        unsigned kb = (unsigned)(uintptr_t)kp + (30 + 2 * r16 + g4) * 16 - wi * 256;
#pragma unroll 1
        for (int r = 0; r < 4; ++r) {
            bf16x8 ua[8], kh[2], kl[2];
            kh[0] = lds_rd128(kb); kl[0] = lds_rd128(kb + 4608);
#pragma unroll
            for (int m = 0; m < 8; ++m) ua[m] = lds_rd128(ub + m * 512);
#pragma unroll
            for (int n = 0; n < 8; ++n) {
                if (n < 7) { kh[(n + 1) & 1] = lds_rd128(kb + (n + 1) * 512); kl[(n + 1) & 1] = lds_rd128(kb + 4608 + (n + 1) * 512); }
#pragma unroll
                for (int m = 0; m + n < 8; ++m) y[m + n] = MFMA16(ua[m], kh[n & 1], y[m + n]);
#pragma unroll
                for (int m = 0; m + n < 8; ++m) y[m + n] = MFMA16(ua[m], kl[n & 1], y[m + n]);
                SBAR();
            }
            ub += 64; kb -= 64;
        }
    }
    ST(12)
    role_sync(cnt, 12, lane);
    {
        char* xb = lds + T_UC + rg * 4096;
#pragma unroll
        for (int j = 0; j < 4; ++j) { const f32x4 ya = y[j], yb = y[4 + j]; *(f32x4*)(xb + j * 1024 + l16) = wi ? ya : yb; }
    }
    role_sync(cnt, 16, lane);
    {
        const char* xb = lds + T_UC + (rg ^ 1) * 4096;
        float* yo = (float*)(lds + T_UC + 16384) + ch * 2048 + wi * 1024 + (lane & 15) * 16 + (lane >> 4) * 4;
#pragma unroll
        for (int j = 0; j < 4; ++j) {
            const f32x4 ya = y[j], yb = y[4 + j];
            *(f32x4*)(yo + j * 256) = (wi ? yb : ya) + *(const f32x4*)(xb + j * 1024 + l16);
        }
    }
    role_sync(cnt, 20, lane);
    ST(13)
    {
        const unsigned dsk = *(const unsigned*)(lds + T_PRM + 3076);
        const float ds0 = __uint_as_float(dsk << 16), ds1 = __uint_as_float(dsk & 0xffff0000u);
        const float* ya = (const float*)(lds + T_UC + 16384);
#pragma unroll 2
        for (int k = 0; k < 8; ++k) {
            const int l = t8 + 256 * k, yi = l;
            const unsigned upk = *(const unsigned*)(lds + T_U2 + l * 4);
            const float u0 = __uint_as_float(upk << 16), u1 = __uint_as_float(upk & 0xffff0000u);
            const float o0 = bfround(ya[yi]) + bfround(u0 * ds0);
            const float o1 = bfround(ya[2048 + yi]) + bfround(u1 * ds1);
            *(unsigned*)(out + (size_t)l * 1024 + 512 + d0) = cvtpk(o0, o1);
        }
    }
    ST_END()
}

__global__ __launch_bounds__(512, 2) void mega_kernel(cbf x, cbf ckv, cbf wq, cbf wuk, cbf wuv, cbf wo, cbf u, cbf are, cbf aim, cbf bre,
                                                      cbf bim, cbf cre, cbf cim, cbf logdt, cbf dskip, char* __restrict__ ws,
                                                      unsigned short* __restrict__ out) {
    __shared__ __attribute__((aligned(16))) char lds[163840];
    u64 pc;
    asm volatile("s_getpc_b64 %0" : "=s"(pc));
    const int tid = threadIdx.x, lane = tid & 63, w = rfl(tid >> 6), r32 = lane & 31, hh = lane >> 5;
    const int b = blockIdx.x, xcd = b & 7, idx = b >> 3, h = xcd * 4 + (idx & 3), rb = idx >> 2;
    const int bi = rb, bj = 15 - rb, grp = w >> 2, rg = w & 3;
    const unsigned l16 = lane * 16;
    gu64* epoch = (gu64*)(ws + WS_FLAG);
    gu64* acks = (gu64*)(ws + WS_FLAG + 4096);
    gu32* ctr = (gu32*)(ws + WS_FLAG + 8192);
    AT c;
    c.ws = ws; c.h = h; c.bi = bi; c.bj = bj; c.grp = grp; c.rg = rg; c.qrel = (lane & 15) - 4 * (lane >> 4); c.w = w;
    c.lb = (unsigned)(uintptr_t)lds; c.l16 = l16;
    c.ldp = ws + WS_KV + ((size_t)h << 20) + w * 4096;
    c.qh = ws + WS_Q + ((size_t)b << 15);
    c.ldbase = c.lb + (grp ? 0 : 98304) + rg * 4096;
    c.ldoff = 0; c.ldlim = grp ? 98304 : 65536; c.koff = 16384; c.voff = 0; c.taun = 1; c.ldleft = 32 - 2 * bi;
    {
    constexpr unsigned S0 = 0u, S1 = 32768u, S2 = 65536u, S3 = 98304u, S4 = 131072u;
    PJ p;
    p.lb = c.lb;
    {
        const int r8 = lane >> 3, sl8 = lane & 7, r4 = lane >> 4, sl16 = lane & 15;
        p.xoff0 = r8 * 1024 + ((sl8 ^ (r8 >> 1)) << 4);
        p.xoff1 = r8 * 1024 + ((sl8 ^ (4 + (r8 >> 1))) << 4);
        p.woff[0] = r4 * 8192 + ((sl16 ^ ((r4 << 2) | ((2 * w) & 3))) << 4);
        p.woff[1] = r4 * 8192 + ((sl16 ^ ((r4 << 2) | ((2 * w + 1) & 3))) << 4);
    }
    p.xdst = p.lb + w * 4096; p.wdst = p.lb + w * 2048;
    const size_t wrow_off = (size_t)(w * 8) * 8192 + h * 256;
    p.xs = (const char*)ckv + (size_t)((rg * 8 + rb) * 64 + grp * 32) * 1024; p.w1 = (const char*)wuk + wrow_off; p.w2 = (const char*)wuv + wrow_off;
#define XADV() p.xs += 128
#define WADV() { p.w1 += 64 * 8192; p.w2 += 64 * 8192; }
    const int sh = idx * 8 + w;
    const size_t warm_x = (size_t)(sh * 8 + (lane & 7)) * 1024 + ((lane >> 3) & 1) * 128;
    const size_t warm_w = (size_t)(sh >> 1) * 8192 + xcd * 1024 + ((sh & 1) * 4 + (lane & 3)) * 128;
    p.winc = lane < 16 ? 256u : 128u * 8192u;
    p.wp = (lane < 16 ? (const char*)ckv + warm_x : (lane < 20 ? (const char*)wuk : (const char*)wuv) + warm_w) + (size_t)p.winc;
    p.dummy = 0;
    const unsigned vzero = 0;
    u64 ep;
    asm volatile("global_load_dwordx2 %0, %1, %2 sc1" : "=v"(ep) : "v"(vzero), "s"(epoch) : "memory");
    if (b == 0 && w == 0) {
#pragma unroll
        for (int k = 0; k < 3; ++k) if (k * 64 + lane < C_NUM) __hip_atomic_store(ctr + (k * 64 + lane) * 32, 0u, RLX_AGENT);
    }
#pragma unroll
    for (int t = 0; t < 2; ++t) {
#pragma unroll
        for (int q = 0; q < 4; ++q) { pj_x(p, q, t ? S1 : S0); pj_w(p, q, t ? S3 : S2); }
        XADV(); WADV();
    }
    pj_warm(p);
    {

        const unsigned off = (sh * 3 + lane) * 128;
        asm volatile("v_cmp_gt_u32 vcc, %3, %1\n\ts_and_b64 exec, vcc, 7\n\ts_nop 4\n\tglobal_load_dword %0, %1, %2\n\ts_mov_b64 exec, -1"
                     : "+v"(p.dummy) : "v"(off), "s"(pc & ~127ull), "s"((unsigned)CODEWARM) : "memory", "vcc", "scc");
    }
    asm volatile("s_waitcnt vmcnt(10)" ::: "memory");
    PIN(ep);
    ep = ((u64)rfl((unsigned)(ep >> 32)) << 32) | rfl((unsigned)ep);
    if (tid == 0) __hip_atomic_store(acks + b, ack_of(ep, b), RLX_AGENT);
    ST(14)
    const int r16 = lane & 15, g4 = lane >> 4, q4 = r16 >> 2, p4 = lane & 3;
#define WBASE(cc, t) (p.lb + (8 * g4 + 4 * (t) + q4) * 256 + (((2 * (cc) + (p4 >> 1)) ^ ((q4 << 2) | ((g4 & 1) << 1) | (t))) << 4) + (p4 & 1) * 8)
    const int th = w >> 2, dq = w & 3;
    const int tq = w >> 1, dh = w & 1;
#pragma unroll
    for (int s = 0; s < 2; ++s) p.xa[s] = p.lb + th * 16384 + r16 * 128 + (((4 * s + g4) ^ ((r16 >> 1) & 7)) << 4);
#pragma unroll
    for (int i = 0; i < 4; ++i) p.wa[i >> 1][i & 1] = WBASE(2 * dq + (i >> 1), i & 1);
    SYNC_LDS();
    ST(2)
    FW f0, f1;
    bf16x8 xr[8];
    bf16x8 kvp[16];
    {
        f32x4 acck[8][2], accv[8][2];
#pragma unroll
        for (int i = 0; i < 16; ++i) { acck[i >> 1][i & 1] = f32x4{}; accv[i >> 1][i & 1] = f32x4{}; }
#pragma unroll
        for (int j = 0; j < 8; ++j) kvw_read(p, f0, j, 0, S2);
#pragma unroll
        for (int j = 0; j < 4; ++j) xr[j] = x_read(p, j, 0, S0);
#define KV_QUARTER(Q, fc, fn, xb, wb, xn, wn, RD, H1, H2, H3)                                                              \
        _Pragma("unroll") for (int gi = 0; gi < 4; ++gi) {                                                                \
            const int G = 4 * (Q) + gi, tt = G & 7, N = G + 4;                                                            \
            if (N < 16) xr[N & 7] = x_read(p, N & 7, N >> 3, xb); else if (RD) xr[N & 7] = x_read(p, N & 7, 0, xn);       \
            acck[tt][0] = MFMA16(fw(fc, 0), xr[tt], acck[tt][0]); PIN(acck[tt][0]);                                       \
            SBAR();                                                                                                       \
            if ((Q) < 2) kvw_read(p, fn, G, 1, wb); else if ((Q) == 3 && RD) kvw_read(p, fn, 2 * gi, 0, wn);              \
            acck[tt][1] = MFMA16(fw(fc, 1), xr[tt], acck[tt][1]); PIN(acck[tt][1]);                                       \
            SBAR();                                                                                                       \
            if ((Q) == 3 && RD) kvw_read(p, fn, 2 * gi + 1, 0, wn);                                                       \
            accv[tt][0] = MFMA16(xr[tt], fw(fc, 2), accv[tt][0]); PIN(accv[tt][0]);                                       \
            SBAR();                                                                                                       \
            accv[tt][1] = MFMA16(xr[tt], fw(fc, 3), accv[tt][1]); PIN(accv[tt][1]);                                       \
            if (gi == 0) { H1; } if (gi == 2) { H2; } if (gi == 3) { H3; }                                                \
            SBAR();                                                                                                       \
        }
        KV_QUARTER(0, f0, f1, S0, S2, 0u, 0u, true, pj_warm(p), , )
        KV_QUARTER(1, f0, f1, S0, S2, 0u, 0u, true, pj_w(p, 0, S4), pj_w(p, 1, S4), )
        KV_QUARTER(2, f1, f0, S0, S2, 0u, 0u, true, pj_w(p, 2, S4), pj_w(p, 3, S4), WADV())
        SYNC_N(5);
        KV_QUARTER(3, f1, f0, S0, S2, S1, S3, true, pj_x(p, 0, S0), pj_x(p, 1, S0), )
        unsigned wcur = S3, wnext = S4, wld = S2;
#pragma unroll 1
        for (int t = 1; t < 6; ++t) {
            const unsigned xb = (t & 1) << 15, xn = xb ^ S1;
            if (t == 2) p.wp = lane < 16 ? (const char*)x + warm_x : (const char*)wq + warm_w;
            if (t == 2 && b == 0 && w == 0) {
                unsigned spins = 0;
                for (;;) {
                    bool ok = true;
#pragma unroll
                    for (int k = 0; k < 4; ++k) ok &= __hip_atomic_load(acks + k * 64 + lane, RLX_AGENT) == ack_of(ep, k * 64 + lane);
                    if (__all(ok) || ++spins >= SPIN_CAP) break;
                    __builtin_amdgcn_s_sleep(2);
                }
                asm volatile("s_waitcnt vmcnt(0)" ::: "memory");
                if (lane == 0) __hip_atomic_store(epoch, ep + 1, RLX_AGENT);
            }
            KV_QUARTER(0, f0, f1, xb, wcur, 0u, 0u, true, pj_x(p, 2, xn), pj_x(p, 3, xn); XADV(), pj_warm(p))
            KV_QUARTER(1, f0, f1, xb, wcur, 0u, 0u, true, pj_w(p, 0, wld), pj_w(p, 1, wld), )
            KV_QUARTER(2, f1, f0, xb, wcur, 0u, 0u, true, pj_w(p, 2, wld), pj_w(p, 3, wld), WADV())
            SYNC_N(5);
            KV_QUARTER(3, f1, f0, xb, wcur, xn, wnext, true, pj_x(p, 0, xb), pj_x(p, 1, xb), )
            const unsigned tmp = wcur; wcur = wnext; wnext = wld; wld = tmp;
        }
        SBAR();
#define WO_DMA(q) glds1((const char*)wo + (size_t)((b >> 1) * 32 + (w * 2 + (q)) * 2) * 1024 + (b & 1) * 512, (lane >> 5) * 1024 + (lane & 31) * 16, p.lb + S4 + 16384 + (w * 2 + (q)) * 1024)
        p.w1 = (const char*)wq + wrow_off;
        KV_QUARTER(0, f0, f1, S0, S2, 0u, 0u, true, pj_x(p, 2, S1), pj_x(p, 3, S1), )
        p.xs = (const char*)x + (size_t)((grp ? bj : bi) * 128 + rg * 32) * 1024;
        KV_QUARTER(1, f0, f1, S0, S2, 0u, 0u, true, pj_w(p, 0, S4), pj_w(p, 1, S4), WADV())
        KV_QUARTER(2, f1, f0, S0, S2, 0u, 0u, true, WO_DMA(0), WO_DMA(1), )
        SYNC_N(4);
        KV_QUARTER(3, f1, f0, S0, S2, S1, S3, true, pj_x(p, 0, S0), pj_x(p, 1, S0), )
        KV_QUARTER(0, f0, f1, S1, S3, 0u, 0u, true, pj_x(p, 2, S0), pj_x(p, 3, S0), XADV())
        KV_QUARTER(1, f0, f1, S1, S3, 0u, 0u, true, pj_w(p, 0, S2), pj_w(p, 1, S2), WADV())
        KV_QUARTER(2, f1, f0, S1, S3, 0u, 0u, true, pj_w(p, 0, S2 + 16384), pj_w(p, 1, S2 + 16384), WADV())
        SYNC_N(4);
        asm volatile("" :: "v"(p.dummy));
        KV_QUARTER(3, f1, f0, S1, S3, 0u, 0u, false, pj_x(p, 0, S1), pj_x(p, 1, S1), )
        ST(3)
#pragma unroll
        for (int i = 0; i < 8; ++i) p.wa[i >> 1][i & 1] = WBASE(4 * dh + (i >> 1), i & 1);
#pragma unroll
        for (int s = 0; s < 2; ++s) p.xa[s] += tq * 8192 - th * 16384;
#pragma unroll
        for (int j = 0; j < 8; ++j) qw_read(p, f0, j, 0, S4);
#pragma unroll
        for (int j = 0; j < 2; ++j) xr[j] = x_read(p, j, 0, S0);
#pragma unroll
        for (int tb = 0; tb < 4; ++tb)
#pragma unroll
            for (int i = 0; i < 2; ++i) {
                kvp[tb * 4 + i * 2] = pack2(acck[2 * tb + i][0], acck[2 * tb + i][1]);
                kvp[tb * 4 + i * 2 + 1] = pack2(accv[2 * tb][i], accv[2 * tb + 1][i]);
                PIN(kvp[tb * 4 + i * 2]); PIN(kvp[tb * 4 + i * 2 + 1]);
            }
    }
    ST(4)
    {
        f32x4 acc[4][4];
#pragma unroll
        for (int i = 0; i < 16; ++i) acc[i >> 2][i & 3] = f32x4{};
        char* kvt = ws + WS_KV + ((size_t)(h * 32 + rb) << 15);
#define KVST(i) st16(kvt + ((size_t)((i) >> 2) << 18) + (((i) & 1) ? 16384 + (((dq * 2 + (((i) >> 1) & 1)) * 2 + th) << 10)            \
                                                                  : (((th * 2 + (((i) >> 1) & 1)) * 4 + dq) << 10)), l16, kvp[i])
#define KVSIG(g) if (w == 0) atomic_inc1(ws, C_KV + h * 4 + (g), l16, 1u);
#define WO_ST(it) { const int f = w + (it) * 8; const unsigned a = p.lb + S4 + 16384 + (4 * g4 + q4) * 512 + (f >> 2) * 128 + p4 * 32 + (f & 3) * 8;   \
                    st16(ws + WS_WO + ((size_t)(((b & 1) * 16 + f) * 128 + (b >> 1)) << 10), l16, cat8(lds_tr(a), lds_tr(a + 8192))); }
#define U_DMA() glds1((const char*)u + (size_t)((b >> 2) * 32 + w * 4) * 1024 + (b & 3) * 256, (lane >> 4) * 1024 + (lane & 15) * 16, p.lb + S4 + 16384 + w * 1024)
#define U_ST() { const char* us = lds + S4 + 16384 + (tid & 7) * 1024 + (tid >> 3) * 4;                                                  \
                 const u32x4 v = {*(const unsigned*)us, *(const unsigned*)(us + 256), *(const unsigned*)(us + 512), *(const unsigned*)(us + 768)};   \
                 st16(ws + WS_UT + (size_t)((b & 3) * 64) * 8192 + (b >> 2) * 128, (tid >> 3) * 8192 + (tid & 7) * 16, __builtin_bit_cast(bf16x8, v)); }
#define Q_STEP(Q, fc, fn, xb, wb, xn, wn, RD, H0, H1, H2, H3)                                                             \
        _Pragma("unroll") for (int gi = 0; gi < 2; ++gi) {                                                                \
            const int G = 2 * (Q) + gi, tt = G & 3, N = G + 2;                                                            \
            if (N < 8) xr[N & 3] = x_read(p, N & 3, N >> 2, xb); else if (RD) xr[N & 3] = x_read(p, N & 3, 0, xn);        \
            _Pragma("unroll") for (int j = 0; j < 4; ++j) {                                                               \
                if ((Q) < 2) { if (j & 1) qw_read(p, fn, 2 * G + (j >> 1), 1, opaque(wb)); }                              \
                else if ((Q) == 3 && RD) qw_read(p, fn, 4 * gi + j, 0, opaque(wn));                                       \
                acc[tt][j] = MFMA16(fw(fc, j), xr[tt], acc[tt][j]); PIN(acc[tt][j]);                                      \
                if (j == 1) { if (gi == 0) { H0; } else { H2; } } if (j == 3) { if (gi == 0) { H1; } else { H3; } }       \
                SBAR();                                                                                                   \
            }                                                                                                             \
        }
        constexpr unsigned W0 = S4, W1 = S2, W2 = S2 + 16384;
        u64 ep2; unsigned kvc;
        asm volatile("global_load_dwordx2 %0, %1, %2 sc1" : "=v"(ep2) : "v"(vzero), "s"(epoch) : "memory");
        Q_STEP(0, f0, f1, S0, W0, 0u, 0u, true, KVST(0); KVST(1), KVST(2); KVST(3), pj_x(p, 2, S1), pj_x(p, 3, S1); XADV())
        Q_STEP(1, f0, f1, S0, W0, 0u, 0u, true, pj_x(p, 0, S3), , pj_x(p, 1, S3), )
        Q_STEP(2, f1, f0, S0, W0, 0u, 0u, true, pj_x(p, 2, S3), , pj_x(p, 3, S3); XADV(), )
        SYNC_N(4);
        PIN(ep2);
        if (w == 0) {
            unsigned spins = 0;
            while (ep2 != ep + 1 && ++spins < SPIN_CAP) { __builtin_amdgcn_s_sleep(2); ep2 = __hip_atomic_load(epoch, RLX_AGENT); }
        }
        KVSIG(0)
        Q_STEP(3, f1, f0, S0, W0, S1, W1, true, pj_x(p, 0, S0), , pj_x(p, 1, S0), )
        Q_STEP(0, f0, f1, S1, W1, 0u, 0u, true, pj_x(p, 2, S0), , pj_x(p, 3, S0); XADV(), )
        Q_STEP(1, f0, f1, S1, W1, 0u, 0u, true, pj_w(p, 0, W0), , KVST(4), WO_ST(0))
        Q_STEP(2, f1, f0, S1, W1, 0u, 0u, true, pj_w(p, 1, W0); WADV(), , , WO_ST(1))
        SYNC_N(9);
        Q_STEP(3, f1, f0, S1, W1, S3, W2, true, pj_x(p, 0, S1), , pj_x(p, 1, S1), )
        Q_STEP(0, f0, f1, S3, W2, 0u, 0u, true, pj_x(p, 2, S1), , pj_x(p, 3, S1); XADV(), )
        Q_STEP(1, f0, f1, S3, W2, 0u, 0u, true, pj_w(p, 0, W1), , KVST(5), )
        Q_STEP(2, f1, f0, S3, W2, 0u, 0u, true, pj_w(p, 1, W1); WADV(), , , )
        SYNC_N(8);
        Q_STEP(3, f1, f0, S3, W2, S0, W0, true, pj_x(p, 0, S3), , pj_x(p, 1, S3), U_DMA())
        Q_STEP(0, f0, f1, S0, W0, 0u, 0u, true, pj_x(p, 2, S3), , pj_x(p, 3, S3); XADV(), )
        Q_STEP(1, f0, f1, S0, W0, 0u, 0u, true, pj_w(p, 0, W2), , KVST(6), )
        Q_STEP(2, f1, f0, S0, W0, 0u, 0u, true, pj_w(p, 1, W2); WADV(), , , )
        SYNC_N(8);
        Q_STEP(3, f1, f0, S0, W0, S1, W1, true, pj_x(p, 0, S0), , pj_x(p, 1, S0), )
        asm volatile("global_load_dword %0, %1, %2 sc1" : "=v"(kvc) : "v"((unsigned)(C_KV + h * 4) * 128u), "s"(ws + WS_FLAG + 8192) : "memory");
        Q_STEP(0, f0, f1, S1, W1, 0u, 0u, true, pj_x(p, 2, S0), , pj_x(p, 3, S0); XADV(), )
        Q_STEP(1, f0, f1, S1, W1, 0u, 0u, true, pj_w(p, 0, W0), , KVST(7), )
        Q_STEP(2, f1, f0, S1, W1, 0u, 0u, true, pj_w(p, 1, W0); WADV(), , , )
        SYNC_N(5);
        PIN(kvc);
        if (w == 0) {
            if (rfl(kvc) < 8u) wait_ge(ctr + (C_KV + h * 4) * 32, 8u);
        }
        Q_STEP(3, f1, f0, S1, W1, S3, W2, true, pj_x(p, 0, S1), , pj_x(p, 1, S1), )
        Q_STEP(0, f0, f1, S3, W2, 0u, 0u, true, pj_x(p, 2, S1), , pj_x(p, 3, S1), )
        Q_STEP(1, f0, f1, S3, W2, 0u, 0u, true, pj_w(p, 0, W1), , , )
        Q_STEP(2, f1, f0, S3, W2, 0u, 0u, true, pj_w(p, 1, W1), , , )
        SYNC_N(6);
        KVSIG(1)
#define KSITE(q) if (!grp) { at_piece(c, (q) & 1); if ((q) & 1) at_adv(c); }
#define ASITE(q) { at_piece(c, (q) & 1); if ((q) & 1) at_adv(c); }
        Q_STEP(3, f1, f0, S3, W2, S0, W0, true, KSITE(0), , , )
        const int d0 = xcd * 64 + idx * 2;
        const char* s4p[2];
#pragma unroll
        for (int k = 0; k < 2; ++k) {
            const int e = tid + 512 * k, ar = e >> 7;
            const char* bp = ar == 0 ? (const char*)are : ar == 1 ? (const char*)aim : ar == 2 ? (const char*)bre : ar == 3 ? (const char*)bim :
                             ar == 4 ? (const char*)cre : (const char*)cim;
            s4p[k] = e < 768 ? bp + d0 * 256 + (e & 127) * 4 : (e == 768 ? (const char*)logdt : (const char*)dskip) + d0 * 2;
        }
#define S4P(k) asm volatile("global_load_dword %0, %1, off" : "=v"(c.s4[k]) : "v"(s4p[k]) : "memory")
        Q_STEP(0, f0, f1, S0, W0, 0u, 0u, true, KSITE(1), , KVST(8), )
        Q_STEP(1, f0, f1, S0, W0, 0u, 0u, true, KSITE(2), , U_ST(), )
        Q_STEP(2, f1, f0, S0, W0, 0u, 0u, true, KSITE(3), , KVST(9), )
        if (grp) asm volatile("s_waitcnt vmcnt(3)" ::: "memory"); else asm volatile("s_waitcnt vmcnt(11)" ::: "memory");
        SYNC_LDS();
        Q_STEP(3, f1, f0, S0, W0, S1, W1, true, ASITE(0), , , )
        Q_STEP(0, f0, f1, S1, W1, 0u, 0u, true, ASITE(1), , KVST(10), )
        Q_STEP(1, f0, f1, S1, W1, 0u, 0u, true, ASITE(2), , KVST(11), )
        Q_STEP(2, f1, f0, S1, W1, 0u, 0u, true, ASITE(3), S4P(0), , S4P(1))
        SYNC_LDS();
        Q_STEP(3, f1, f0, S1, W1, 0u, 0u, false, , , , )
        ST(5)
        asm volatile("" :: "v"(p.dummy));
#pragma unroll
        for (int tt = 0; tt < 4; ++tt) {
            const int tk = tq * 2 + (tt >> 1);
            char* xq = lds + S1 + tk * 8192 + l16;
#pragma unroll
            for (int i = 0; i < 2; ++i) {
                const int sfr = (tt & 1) * 4 + dh * 2 + i;
                const bf16x8 v = pack2(acc[tt][2 * i] * QSCALE, acc[tt][2 * i + 1] * QSCALE);
                *(bf16x8*)(xq + sfr * 1024) = v;
                if (tq >= 2) *(bf16x8*)(ws + WS_Q + ((size_t)b << 15) + (tk - 4) * 8192 + sfr * 1024 + l16) = v;
            }
        }
        if (grp) asm volatile("s_waitcnt vmcnt(16)" ::: "memory"); else asm volatile("s_waitcnt vmcnt(13)" ::: "memory");
        SYNC_LDS();
        if (w == 0) atomic_inc1(ws, C_WO, l16, 1u);
        KVST(12); KVST(13); KVST(14); KVST(15);
    }
    }
    const int blk1 = grp ? bj : bi;
    c.kvoff = (C_KV + h * 4 + 1) * 128; c.kvneed = 32 - 2 * bi - 8 + 3; c.kvc = 0;
    c.sigid = C_KV + h * 4 + 2; c.sign = 3;
#pragma unroll
    for (int s = 0; s < 8; ++s) c.qf[s >> 2][s & 3] = lds_rd128(c.lb + 32768 + w * 8192 + s * 1024 + l16);
#pragma unroll
    for (int qt = 0; qt < 2; ++qt) {
        c.negm[qt] = f32x4{}; c.mref[qt] = 0.f; c.ls[qt] = f32x4{}; PIN(c.mref[qt]);
#pragma unroll
        for (int i = 0; i < 8; ++i) c.o[i][qt] = f32x4{};
    }
    c.ones = __builtin_bit_cast(bf16x8, u32x4{0x3f803f80u, 0x3f803f80u, 0x3f803f80u, 0x3f803f80u}); PIN(c.ones);
    SB sA, sB;
    {
        const unsigned ks_ = c.lb + 98304 + l16;
#pragma unroll
        for (int i = 0; i < 16; ++i) {
            const bf16x8 kf = lds_rd128(ks_ + kitem(i));
            const int kt = i & 3, ds = i >> 2;
            sA.s[kt][0] = MFMA16(kf, c.qf[0][ds], ds ? sA.s[kt][0] : f32x4{});
            sA.s[kt][1] = MFMA16(kf, c.qf[1][ds], ds ? sA.s[kt][1] : f32x4{});
        }
#pragma unroll
        for (int i = 0; i < NPRE; ++i) c.fr[i] = lds_rd128(ks_ + 16384 + kitem(i));
        at_mask(c, sA, 0, blk1);
        at_check(c, sA, 0.f, true);
    }
    ST(6)
#define PIN_STATE() { PIN(c.mref[0]); PIN(c.mref[1]); }
#pragma unroll 1
    for (int t = 0; t < bi; ++t) {
        at_step<0>(c, sA, sB, 1, 1, blk1, 0);
        at_step<0>(c, sB, sA, 1, 1, blk1, 0);
    }
    PIN_STATE()
    at_step<1>(c, sA, sB, 1, 1 + grp, blk1, 1);
    at_step<2>(c, sB, sA, 1 + grp, 2, bj, 1);
    PIN_STATE()
#pragma unroll 1
    for (int t = 0; t < 7 - bi; ++t) {
        at_step<0>(c, sA, sB, 2, 2, bj, 1);
        at_step<0>(c, sB, sA, 2, 2, bj, 1);
    }
    PIN_STATE()
    unsigned ticket = 0;
    if (w == 0 && c.l16 == 0) ticket = __hip_atomic_fetch_add(ctr + (C_TICK + xcd) * 32, 1u, RLX_AGENT);
    at_x<false>(c, sA, sB);
    at_bar<false>(c);
    tail_part(c, sB, lds, ticket DIAGARGS);
}

extern "C" void kernel_launch(void* const* d_in, const int* in_sizes, int n_in, void* d_out, int out_size, void* d_ws,
                              size_t ws_size, hipStream_t stream) {
    (void)in_sizes; (void)n_in; (void)out_size; (void)ws_size;
    const unsigned short* const* in = (const unsigned short* const*)d_in;
    mega_kernel<<<256, 512, 0, stream>>>(in[0], in[1], in[2], in[3], in[4], in[5], in[6], in[7], in[8], in[9], in[10], in[11],
                                         in[12], in[13], in[14], (char*)d_ws, (unsigned short*)d_out);
}
```

```cpp
#include <hip/hip_runtime.h>
#include <hip/hip_bf16.h>
#include <stdint.h>

typedef __bf16 bf16x8 __attribute__((ext_vector_type(8)));
typedef __bf16 bf16x2 __attribute__((ext_vector_type(2)));
typedef short s16x4 __attribute__((ext_vector_type(4)));
typedef float f32x4 __attribute__((ext_vector_type(4)));
typedef float f32x2 __attribute__((ext_vector_type(2)));
typedef unsigned u32x4 __attribute__((ext_vector_type(4)));
typedef unsigned long long u64;
typedef const unsigned short* __restrict__ cbf;
typedef __attribute__((address_space(1))) unsigned gu32;
typedef __attribute__((address_space(1))) u64 gu64;
#define LAS __attribute__((address_space(3)))
#define DI __device__ __forceinline__
#define MFMA16(a, b, c) __builtin_amdgcn_mfma_f32_16x16x32_bf16((a), (b), (c), 0, 0, 0)
#define RLX_AGENT __ATOMIC_RELAXED, __HIP_MEMORY_SCOPE_AGENT
#define SBAR() __builtin_amdgcn_sched_barrier(0)
#define PIN(x) asm volatile("" : "+v"(x))
#define MX3(a, b, c) __builtin_fmaxf(__builtin_fmaxf((a), (b)), (c))

constexpr float QSCALE = 0.08838834764831845f * 1.4426950408889634f;
#define THRV 40.0f
constexpr float THR = THRV;
constexpr unsigned SPIN_CAP = 1u << 22;
constexpr unsigned CODEWARM = 61440;

constexpr size_t WS_Q = 0;
constexpr size_t WS_UT = 8u << 20;
constexpr size_t WS_KV = 16u << 20;
constexpr size_t WS_CTX = 48u << 20;
constexpr size_t WS_WO = 64u << 20;
constexpr size_t WS_FLAG = 72u << 20;
constexpr int C_KV = 0, C_CTX = 128, C_WO = 144, C_TICK = 145, C_NUM = 153;

DI unsigned cvtpk(float lo, float hi) { f32x2 v = {lo, hi}; bf16x2 b = __builtin_convertvector(v, bf16x2); return __builtin_bit_cast(unsigned, b); }
DI float bf2f(unsigned short b) { return __uint_as_float(((unsigned)b) << 16); }
DI float bfround(float x) { __bf16 b = (__bf16)x; return (float)b; }
DI unsigned rfl(unsigned x) { return __builtin_amdgcn_readfirstlane(x); }
DI bf16x8 pack2(f32x4 x, f32x4 y) {
    u32x4 w = {cvtpk(x[0], x[1]), cvtpk(x[2], x[3]), cvtpk(y[0], y[1]), cvtpk(y[2], y[3])};
    return __builtin_bit_cast(bf16x8, w);
}
DI float gmax(float x) {
    auto s = __builtin_amdgcn_permlane16_swap(__float_as_uint(x), __float_as_uint(x), false, false);
    x = fmaxf(__uint_as_float(s[0]), __uint_as_float(s[1]));
    auto t = __builtin_amdgcn_permlane32_swap(__float_as_uint(x), __float_as_uint(x), false, false);
    return fmaxf(__uint_as_float(t[0]), __uint_as_float(t[1]));
}
DI bf16x8 lds_rd128(unsigned addr) { return *(const LAS bf16x8*)(uintptr_t)addr; }
DI s16x4 lds_tr(unsigned addr) { return __builtin_amdgcn_ds_read_tr16_b64_v4i16((LAS s16x4*)(uintptr_t)addr); }
DI bf16x8 cat8(s16x4 lo, s16x4 hi) { return __builtin_bit_cast(bf16x8, __builtin_shufflevector(lo, hi, 0, 1, 2, 3, 4, 5, 6, 7)); }

DI void glds1(const void* sbase, unsigned voff, unsigned m0v) {
    unsigned keep;
    asm volatile("s_mov_b32 %0, m0\n\ts_mov_b32 m0, %3\n\ts_nop 0\n\tglobal_load_lds_dwordx4 %1, %2\n\ts_mov_b32 m0, %0"
                 : "=&s"(keep) : "v"(voff), "s"(sbase), "s"(m0v) : "memory");
}
template <int OFF> DI void glds2m(const void* sbase, unsigned voff, unsigned m0v, int left) {
    unsigned keep;
    asm volatile("s_mov_b32 %0, m0\n\ts_mov_b32 m0, %3\n\ts_cmp_gt_i32 %4, 0\n\ts_cselect_b64 exec, -1, 0\n\t"
                 "global_load_lds_dwordx4 %1, %2 offset:%5 sc1\n\tglobal_load_lds_dwordx4 %1, %2 offset:%6 sc1\n\ts_mov_b64 exec, -1\n\ts_mov_b32 m0, %0"
                 : "=&s"(keep) : "v"(voff), "s"(sbase), "s"(m0v), "s"(left), "i"(OFF), "i"(OFF + 1024) : "memory", "scc");
}
DI void st16(char* ubase, unsigned voff, bf16x8 v) {
    __amdgpu_buffer_rsrc_t r = __builtin_amdgcn_make_buffer_rsrc(ubase, (short)0, 0x7fffffff, 0x00020000);
    __builtin_amdgcn_raw_buffer_store_b128(__builtin_bit_cast(u32x4, v), r, voff, 0, 16);
}
DI f32x4 mfma_c(bf16x8 a, bf16x8 b, const f32x4& cc) {
    f32x4 d;
    asm volatile("v_mfma_f32_16x16x32_bf16 %0, %1, %2, %3" : "=&v"(d) : "v"(a), "v"(b), "v"(cc));
    return d;
}
DI void mfma_acc(f32x4& acc, bf16x8 a, bf16x8 b) { asm volatile("v_mfma_f32_16x16x32_bf16 %0, %1, %2, %0" : "+v"(acc) : "v"(a), "v"(b)); }
#define MFMA_DONE() { SBAR(); asm volatile("s_nop 7"); SBAR(); }
#define SYNC_N(n) asm volatile("s_waitcnt vmcnt(" #n ") lgkmcnt(0)\n\ts_barrier" ::: "memory")
#define SYNC_LDS() asm volatile("s_waitcnt lgkmcnt(0)\n\ts_barrier" ::: "memory")
DI void atomic_inc1(char* ws, unsigned id, unsigned l16, unsigned mask) {
    const unsigned one = 1;
    asm volatile("s_mov_b32 exec_lo, %3\n\ts_mov_b32 exec_hi, 0\n\tglobal_atomic_add %0, %1, %2\n\ts_mov_b64 exec, -1"
                 :: "v"(l16 * 8 + id * 128), "v"(one), "s"(ws + WS_FLAG + 8192), "s"(mask) : "memory");
}
DI void wait_ge(gu32* p, unsigned want) {
    unsigned spins = 0;
    while (rfl(__hip_atomic_load(p, RLX_AGENT)) < want && ++spins < SPIN_CAP) __builtin_amdgcn_s_sleep(2);
}
DI u64 ack_of(u64 e, int wg) { return (e ^ 0x9E3779B97F4A7C15ull) + (u64)(wg + 1) * 0x100000001B3ull; }

DI unsigned opaque(unsigned x) { asm volatile("" : "+s"(x)); return x; }
struct PJ {
    const char* xs; const char* w1; const char* w2;
    const char* wp; unsigned winc, dummy;
    unsigned lb, xoff0, xoff1, woff[2], xdst, wdst;
    unsigned xa[2], wa[4][2];
};
DI void pj_x(const PJ& c, int q, unsigned xb) { glds1(c.xs + q * 8192, (q & 1) ? c.xoff1 : c.xoff0, c.xdst + xb + q * 1024); }
DI void pj_w(const PJ& c, int q, unsigned wb) { glds1((q < 2 ? c.w1 : c.w2) + (q & 1) * 32768, c.woff[q & 1], c.wdst + wb + (q >> 1) * 16384 + (q & 1) * 1024); }
DI void pj_warm(PJ& c) {
    asm volatile("s_mov_b64 exec, 0xffffff\n\tglobal_load_dword %0, %1, off\n\ts_mov_b64 exec, -1" : "+v"(c.dummy) : "v"(c.wp) : "memory");
    c.wp += c.winc;
}
struct FW { s16x4 h[4][2]; };
DI bf16x8 fw(const FW& f, int i) { return cat8(f.h[i][0], f.h[i][1]); }
DI void kvw_read(const PJ& c, FW& f, int j, int s, unsigned wb) { f.h[j >> 1][j & 1] = lds_tr(c.wa[(j >> 1) & 1][j & 1] + wb + (j >> 2) * 16384 + s * 8192); }
DI void qw_read(const PJ& c, FW& f, int j, int s, unsigned wb) { f.h[j >> 1][j & 1] = lds_tr(c.wa[j >> 1][j & 1] + wb + s * 8192); }
DI bf16x8 x_read(const PJ& c, int tt, int s, unsigned xb) { return lds_rd128(c.xa[s] + xb + tt * 2048); }

constexpr int NPRE = 6;
struct SB { f32x4 s[4][2]; };
struct AT {
    char* ws; const char* ldp; const char* qh;
    unsigned sign, sigid;
    unsigned kvoff; int kvneed; unsigned kvc;
    int h, bi, bj, grp, rg, qrel, w;
    unsigned lb, l16;
    unsigned ldoff, ldlim, ldbase;
    unsigned koff, voff;
    int taun, ldleft;
    bf16x8 qf[2][4], fr[8];
    unsigned pw[16];
    f32x4 o[8][2], negm[2];
    f32x4 ls[2];
    bf16x8 ones;
    unsigned s4[2];
    float mref[2];
};
DI void at_piece(AT& c, int q) {
    const unsigned m0v = c.ldbase + c.ldoff;
    if (q == 0) glds2m<0>(c.ldp, c.l16, m0v, c.ldleft); else glds2m<2048>(c.ldp, c.l16, m0v, c.ldleft);
}
DI void at_adv(AT& c, int on = 1) { c.ldp += on * 32768; c.ldoff += on * 16384; if (c.ldoff >= c.ldlim) c.ldoff = 0; c.ldleft -= on; }
DI unsigned kitem(int j) { return ((j & 3) * 4 + (j >> 2)) * 1024; }
DI unsigned vitem(int j) { return ((j & 7) * 2 + (j >> 3)) * 1024; }
#define SEL(S, e) (S).s[((e) >> 4) * 2 + (((e) >> 2) & 1)][((e) >> 3) & 1][(e) & 3]

DI void at_mask(AT& c, SB& s, int tau, int blk) {
    const int row0 = blk * 128 + c.rg * 32;
    if (tau * 64 + 63 > row0) {
        MFMA_DONE()
        const float dq = (float)(row0 + c.qrel - tau * 64);
#pragma unroll
        for (int kt = 0; kt < 4; ++kt)
#pragma unroll
            for (int qt = 0; qt < 2; ++qt)
#pragma unroll
                for (int e = 0; e < 4; ++e)
                    s.s[kt][qt][e] = __builtin_fmaf(__builtin_fminf(dq - (float)(kt * 16 + e - qt * 16), 0.f), 1e30f, s.s[kt][qt][e]);
    }
#pragma unroll
    for (int i = 0; i < 8; ++i) PIN(s.s[i >> 1][i & 1]);
}
DI void at_check(AT& c, SB& s, float ma, bool first) {
    if (__builtin_expect(first || __any(ma > THR), 0)) {
        MFMA_DONE()
#pragma unroll
        for (int qt = 0; qt < 2; ++qt) {
            float mx = MX3(s.s[0][qt][0], s.s[0][qt][1], s.s[0][qt][2]);
            mx = fmaxf(mx, s.s[0][qt][3]);
#pragma unroll
            for (int kt = 1; kt < 4; ++kt) { mx = MX3(mx, s.s[kt][qt][0], s.s[kt][qt][1]); mx = MX3(mx, s.s[kt][qt][2], s.s[kt][qt][3]); }
            mx = gmax(mx);
            const float dl = first ? mx : __builtin_fmaxf(mx, 0.f);
            c.mref[qt] += dl; PIN(c.mref[qt]);

#pragma unroll
            for (int kt = 0; kt < 4; ++kt) s.s[kt][qt] -= dl;
            c.negm[qt] = f32x4{-c.mref[qt], -c.mref[qt], -c.mref[qt], -c.mref[qt]};
            PIN(c.negm[qt]);
            if (!first) {
                const float fs = __builtin_amdgcn_exp2f(-dl);
                c.ls[qt] *= fs;
#pragma unroll
                for (int d = 0; d < 8; ++d) c.o[d][qt] *= fs;
            }
        }
        asm volatile("s_nop 1");
    }
#pragma unroll
    for (int i = 0; i < 8; ++i) PIN(s.s[i >> 1][i & 1]);
}
template <bool QK> DI void at_x(AT& c, SB& sc, SB& sn) {
    const unsigned ks_ = c.lb + 98304 + c.koff + c.l16, vs_ = c.lb + c.voff + c.l16;
    float e0 = 0.f, e1 = 0.f;
#pragma unroll
    for (int i = 0; i < 16; ++i) {
        const int n = i + NPRE, kt = i & 3, ds = i >> 2;
        if (i == 0) __builtin_amdgcn_s_setprio(1);
        if (i == 8) __builtin_amdgcn_s_setprio(0);
        if (n < 16) { if (QK) c.fr[n & 7] = lds_rd128(ks_ + kitem(n)); }
        else c.fr[n & 7] = lds_rd128(vs_ + vitem(n - 16));
        if (QK) { if (ds) mfma_acc(sn.s[kt][0], c.fr[i & 7], c.qf[0][ds]); else sn.s[kt][0] = mfma_c(c.fr[i & 7], c.qf[0][0], c.negm[0]); }
        if (i > 0) { c.pw[i - 1] = cvtpk(e0, e1); PIN(c.pw[i - 1]); }
        e0 = __builtin_amdgcn_exp2f(SEL(sc, 2 * i)); PIN(e0);
        SBAR();
        if (QK) { if (ds) mfma_acc(sn.s[kt][1], c.fr[i & 7], c.qf[1][ds]); else sn.s[kt][1] = mfma_c(c.fr[i & 7], c.qf[1][0], c.negm[1]); }
        e1 = __builtin_amdgcn_exp2f(SEL(sc, 2 * i + 1)); PIN(e1);
        SBAR();
    }
    c.pw[15] = cvtpk(e0, e1); PIN(c.pw[15]);
}
template <bool POLL> DI void at_bar(AT& c) {
    asm volatile("s_waitcnt vmcnt(0)\n\ts_barrier" ::: "memory");
    if (c.sign) {
        if (c.w == 0) atomic_inc1(c.ws, c.sigid, c.l16, c.sign);
        c.sign = 0;
    }
    PIN(c.kvc);
    if (!POLL) return;
    const bool need = c.ldleft - 2 * c.grp <= c.kvneed && c.kvneed > 3;
    if (need && c.w == 0) {
        if (rfl(c.kvc) < 8u) wait_ge((gu32*)(c.ws + WS_FLAG + 8192 + c.kvoff), 8u);
    }
    c.kvoff += need ? 128 : 0; c.kvneed -= need ? 8 : 0;
    asm volatile("s_cmp_eq_u32 %3, 0\n\ts_cbranch_scc0 .Lskip%=\n\tglobal_load_dword %0, %1, %2 sc1\n.Lskip%=:"
                 : "+v"(c.kvc) : "v"(c.kvoff), "s"(c.ws + WS_FLAG + 8192), "s"(c.w) : "memory", "scc");
}
#define PFRAG(u, qt) __builtin_bit_cast(bf16x8, u32x4{c.pw[((u) * 2 + (qt)) * 4], c.pw[((u) * 2 + (qt)) * 4 + 1], c.pw[((u) * 2 + (qt)) * 4 + 2], c.pw[((u) * 2 + (qt)) * 4 + 3]})
template <bool NEXT, bool QLD> DI float at_y(AT& c, SB& sn, int inc, int blkn, int two) {
    if (NEXT) __builtin_amdgcn_s_setprio(3);
    if (NEXT) at_bar<true>(c);
    if (QLD) {
#pragma unroll
        for (int s = 0; s < 8; ++s) c.qf[s >> 2][s & 3] = *(const bf16x8*)(c.qh + c.rg * 8192 + s * 1024 + c.l16);
    }
    if (NEXT) at_mask(c, sn, c.taun, blkn);
    const unsigned vs_ = c.lb + c.voff + c.l16;
    c.koff = (c.koff + inc * 16384) & 65535;
    const unsigned ks_ = c.lb + 98304 + c.koff + c.l16;
    float ma = -__builtin_inff();
#pragma unroll
    for (int i = 0; i < 16; ++i) {
        const int n = i + NPRE, dt = i & 7, u = i >> 3, lq = (i >> 2) & 1;
        if (NEXT && i == 8) __builtin_amdgcn_s_setprio(2);
        if (n < 16) c.fr[n & 7] = lds_rd128(vs_ + vitem(n));
        else if (NEXT) c.fr[n & 7] = lds_rd128(ks_ + kitem(n - 16));
        mfma_acc(c.o[dt][0], c.fr[i & 7], PFRAG(u, 0));
        if ((i & 3) == 3) mfma_acc(c.ls[lq], c.ones, PFRAG(u, lq));
        SBAR();
        mfma_acc(c.o[dt][1], c.fr[i & 7], PFRAG(u, 1));
        if (NEXT) { ma = MX3(ma, SEL(sn, 2 * i), SEL(sn, 2 * i + 1)); PIN(ma); }
        if ((i & 3) == 1) {
            const int q = i >> 2;
            if (NEXT && !QLD && (q < 2 || two)) { at_piece(c, q & 1); if (q & 1) at_adv(c); }
        }
        SBAR();
    }
    return ma;
}
template <int KIND> DI void at_step(AT& c, SB& sc, SB& sn, int incv, int inck, int blkn, int two) {
    at_x<true>(c, sc, sn);
    const float ma = at_y<true, KIND == 1>(c, sn, inck, blkn, two);
    c.voff += incv * 16384; if (c.voff >= 98304) c.voff -= 98304;
    c.taun += inck;
    if constexpr (KIND == 2) {
        if (!c.grp) {
            MFMA_DONE()
#pragma unroll
            for (int qt = 0; qt < 2; ++qt) {
                const float inv = 1.0f / c.ls[qt][0];
                char* dst = c.ws + WS_CTX + ((size_t)((c.bi * 8 + c.rg * 2 + qt) * 128 + c.h * 4) << 10);
#pragma unroll
                for (int sp = 0; sp < 4; ++sp) st16(dst + sp * 1024, c.l16, pack2(c.o[2 * sp][qt] * inv, c.o[2 * sp + 1][qt] * inv));
#pragma unroll
                for (int k = 0; k < 8; ++k) c.o[k][qt] = f32x4{};
                c.mref[qt] = 0.f; c.ls[qt] = f32x4{}; PIN(c.mref[qt]);
            }
        }
        PIN(c.mref[0]); PIN(c.mref[1]);
        c.sigid = C_CTX + c.bi; c.sign = 1;
        at_check(c, sn, ma, !c.grp);
    } else {
        at_check(c, sn, ma, false);
    }
    if constexpr (KIND == 1) {
#pragma unroll
        for (int s = 0; s < 8; ++s) PIN(c.qf[s >> 2][s & 3]);
#pragma unroll
        for (int q = 0; q < 4; ++q) { at_piece(c, q & 1); if (q & 1) at_adv(c); }
        if (!c.grp) {
            c.negm[0] = f32x4{}; c.negm[1] = f32x4{}; PIN(c.negm[0]); PIN(c.negm[1]); asm volatile("s_nop 1");
        }
    }
}

struct cf { float r, i; };
DI cf cmul(cf a, cf b) { return cf{a.r * b.r - a.i * b.i, a.r * b.i + a.i * b.r}; }
constexpr int UCOPY = 4160;
constexpr int T_U2 = 64;
constexpr int T_PRM = 8448;
constexpr int T_RED = 12032;
constexpr int T_UC = T_RED + 49152;
constexpr int T_KP = T_UC + 2 * 8 * UCOPY;
constexpr int T_KD = T_KP + 2 * 12288;
constexpr int KDB = 2592;
constexpr int T_CNT = T_KD + 4 * KDB;
static_assert(T_CNT + 16 <= 163840 && T_CNT >= T_RED + 8 * 9216, "lds");
DI void role_sync(LAS unsigned* cnt, unsigned target, int lane) {
    asm volatile("s_waitcnt lgkmcnt(0)" ::: "memory");
    if (lane == 0) __hip_atomic_fetch_add(cnt, 1u, __ATOMIC_RELAXED, __HIP_MEMORY_SCOPE_WORKGROUP);
    while (__hip_atomic_load(cnt, __ATOMIC_RELAXED, __HIP_MEMORY_SCOPE_WORKGROUP) < target) __builtin_amdgcn_s_sleep(1);
    asm volatile("" ::: "memory");
}
DI void hilo_store(char* kp, int c8, const float (&kv)[8]) {
    unsigned hi[4], lo[4];
#pragma unroll
    for (int e = 0; e < 4; ++e) {
        hi[e] = cvtpk(kv[2 * e], kv[2 * e + 1]);
        lo[e] = cvtpk(kv[2 * e] - __uint_as_float(hi[e] << 16), kv[2 * e + 1] - __uint_as_float(hi[e] & 0xffff0000u));
    }
    *(u32x4*)(kp + (c8 + 32) * 16) = u32x4{hi[0], hi[1], hi[2], hi[3]};
    *(u32x4*)(kp + 4608 + (c8 + 32) * 16) = u32x4{lo[0], lo[1], lo[2], lo[3]};
}
constexpr int OD = 4;

DI void tail_part(AT& c, SB& sdummy, char* lds, unsigned ticket) {
    unsigned lane_;
    asm volatile("v_mbcnt_lo_u32_b32 %0, -1, 0\n\tv_mbcnt_hi_u32_b32 %0, -1, %0" : "=v"(lane_));
    typedef const void* const __attribute__((address_space(4)))* kargs_t;
    kargs_t ka = (kargs_t)__builtin_amdgcn_kernarg_segment_ptr();
    asm volatile("" : "+s"(ka));
    unsigned short* __restrict__ out = (unsigned short*)ka[16];
    char* ws = c.ws;
    const int lane = lane_, w = c.w, tid = w * 64 + lane, r32 = lane & 31, hh = lane >> 5, grp = c.grp, rg = c.rg, h = c.h, bj = c.bj;
    const unsigned l16 = lane * 16;
    const int b = blockIdx.x, xcd = b & 7, idx = b >> 3;
    gu32* ctr = (gu32*)(ws + WS_FLAG + 8192);
    const int d0 = xcd * 64 + idx * 2;
    unsigned rdy;
    ticket = rfl(ticket);
    const int mb0 = (ticket < 16 ? 0 : 16) + xcd * 2 + ((ticket >> 3) & 1);
    asm volatile("global_load_dword %0, %1, %2 sc1" : "=v"(rdy) : "v"((unsigned)((lane & 1) ? C_CTX + (mb0 >> 1) : C_WO) * 128u), "s"(ws + WS_FLAG + 8192) : "memory");
    SBAR();
    at_y<false, false>(c, sdummy, 0, bj, 0);
    MFMA_DONE()
    LAS unsigned* cnts = (LAS unsigned*)(uintptr_t)(c.lb + T_CNT);
    {
        const float l0 = c.ls[0][0], l1 = c.ls[1][0];
        SYNC_LDS();
        char* mine = lds + T_RED + w * 9216;
#pragma unroll
        for (int d = 0; d < 4; ++d)
#pragma unroll
            for (int qt = 0; qt < 2; ++qt) {
                const f32x4 oa = c.o[d][qt], ob = c.o[4 + d][qt];
                *(f32x4*)(mine + (d * 2 + qt) * 1024 + l16) = grp ? oa : ob;
            }
        *(f32x4*)(mine + 8192 + l16) = f32x4{c.mref[0], l0, c.mref[1], l1};
        PIN(c.s4[0]); PIN(c.s4[1]);
        *(unsigned*)(lds + T_PRM + tid * 4) = c.s4[0];
        if (tid < 258) *(unsigned*)(lds + T_PRM + 2048 + tid * 4) = c.s4[1];
        asm volatile("s_waitcnt vmcnt(0)" : "+v"(rdy) :: "memory");
        if (w == 0) {
            if (rfl(rdy) < 256u) wait_ge(ctr + C_WO * 32, 256u);
            const bool ok = __builtin_amdgcn_readlane(rdy, 1) >= 32u;
            if (lane == 0) { cnts[0] = 0; cnts[1] = 0; cnts[2] = ticket | (ok ? 256u : 0u); cnts[3] = 0; }
            if (lane < 16) *(unsigned*)(lds + lane * 4) = 0u;
        }
        SYNC_LDS();
        const __amdgpu_buffer_rsrc_t ru = __builtin_amdgcn_make_buffer_rsrc(ws + WS_UT + (size_t)(d0 >> 1) * 8192, (short)0, 8192, 0x00020000);
        const u32x4 uv = __builtin_amdgcn_raw_buffer_load_b128(ru, tid * 16, 0, 16);
        const char* oth = lds + T_RED + (w ^ 4) * 9216;
        const f32x4 ml = *(const f32x4*)(oth + 8192 + l16);
#pragma unroll
        for (int qt = 0; qt < 2; ++qt) {
            const float mo = ml[2 * qt], lo = ml[2 * qt + 1], mm = c.mref[qt], lm = qt ? l1 : l0;
            const float m = fmaxf(mm, mo);
            const float fa = __builtin_amdgcn_exp2f(mm - m), fb = __builtin_amdgcn_exp2f(mo - m);
            const float inv = 1.0f / (lm * fa + lo * fb);
            const float ca = fa * inv, cb = fb * inv;
            char* dst = ws + WS_CTX + ((size_t)((bj * 8 + rg * 2 + qt) * 128 + h * 4 + (grp ? 2 : 0)) << 10);
            f32x4 r[4];
#pragma unroll
            for (int d = 0; d < 4; ++d) {
                const f32x4 oa = c.o[d][qt], ob = c.o[4 + d][qt];
                r[d] = (grp ? ob : oa) * ca + *(const f32x4*)(oth + (d * 2 + qt) * 1024 + l16) * cb;
            }
            st16(dst, l16, pack2(r[0], r[1])); st16(dst + 1024, l16, pack2(r[2], r[3]));
        }
        *(u32x4*)(lds + T_U2 + tid * 16) = uv;
    }
    const unsigned tk = rfl(cnts[2]);
    SYNC_LDS();
#define CTX_SIGNAL() { asm volatile("s_waitcnt vmcnt(0)" ::: "memory");                                                          \
        if (lane == 0 && __hip_atomic_fetch_add(cnts + 3, 1u, __ATOMIC_RELAXED, __HIP_MEMORY_SCOPE_WORKGROUP) == 7u)             \
            __hip_atomic_fetch_add(ctr + (C_CTX + bj) * 32, 1u, RLX_AGENT); }
    if (!grp) {
        const int mb = ((tk & 255) < 16 ? 0 : 16) + xcd * 2 + ((tk >> 3) & 1), nb = tk & 7;
        const bool early = tk >= 256;
        if (!early) {
            CTX_SIGNAL()
            if (w == 0) wait_ge(ctr + (C_CTX + (mb >> 1)) * 32, 32u);
            role_sync(cnts, 4, lane);
        }
        __builtin_amdgcn_fence(__ATOMIC_ACQUIRE, "wavefront");
        const __amdgpu_buffer_rsrc_t ra = __builtin_amdgcn_make_buffer_rsrc(ws + WS_CTX + ((size_t)((mb * 4) * 128 + w * 32) << 10), (short)0, 0x7fffffff, 0x00020000);
        const __amdgpu_buffer_rsrc_t rb_ = __builtin_amdgcn_make_buffer_rsrc(ws + WS_WO + ((size_t)((nb * 4) * 128 + w * 32) << 10), (short)0, 0x7fffffff, 0x00020000);
#define LDA(off) __builtin_bit_cast(bf16x8, __builtin_amdgcn_raw_buffer_load_b128(ra, l16, (off), 16))
#define LDB(off) __builtin_bit_cast(bf16x8, __builtin_amdgcn_raw_buffer_load_b128(rb_, l16, (off), 16))
        f32x4 acc[4][4];
#pragma unroll
        for (int i = 0; i < 16; ++i) acc[i >> 2][i & 3] = f32x4{};
        bf16x8 rga[OD][4], rgb[OD][4];
#pragma unroll
        for (int k = 0; k < OD; ++k)
#pragma unroll
            for (int i = 0; i < 4; ++i) { rga[k][i] = LDA(i * 131072 + k * 1024); rgb[k][i] = LDB(i * 131072 + k * 1024); }
        SBAR();
        if (early) {
            asm volatile("s_waitcnt vmcnt(%0)" :: "i"(8 * OD) : "memory");
            if (lane == 0 && __hip_atomic_fetch_add(cnts + 3, 1u, __ATOMIC_RELAXED, __HIP_MEMORY_SCOPE_WORKGROUP) == 7u)
                __hip_atomic_fetch_add(ctr + (C_CTX + bj) * 32, 1u, RLX_AGENT);
        }
        SBAR();
#pragma unroll 1
        for (int k0 = 0; k0 < 32 - OD; k0 += OD) {
#pragma unroll
            for (int k = 0; k < OD; ++k) {
#pragma unroll
                for (int i = 0; i < 16; ++i) acc[i >> 2][i & 3] = MFMA16(rgb[k][i & 3], rga[k][i >> 2], acc[i >> 2][i & 3]);
                SBAR();
                const int kn = (k0 + OD + k) * 1024;
#pragma unroll
                for (int i = 0; i < 4; ++i) { rga[k][i] = LDA(i * 131072 + kn); rgb[k][i] = LDB(i * 131072 + kn); }
                SBAR();
            }
        }
#pragma unroll
        for (int k = 0; k < OD; ++k)
#pragma unroll
            for (int i = 0; i < 16; ++i) acc[i >> 2][i & 3] = MFMA16(rgb[k][i & 3], rga[k][i >> 2], acc[i >> 2][i & 3]);
#pragma unroll
        for (int i = 0; i < 4; ++i) {
            if (i != w) {
                const int slot = w < i ? w : w - 1;
#pragma unroll
                for (int t = 0; t < 4; ++t) *(f32x4*)(lds + T_RED + (i * 3 + slot) * 4096 + t * 1024 + l16) = acc[i][t];
            }
        }
        role_sync(cnts, early ? 4 : 8, lane);
        unsigned pk[8];
#pragma unroll
        for (int t = 0; t < 4; ++t) {
            f32x4 sm = w == 0 ? acc[0][t] : (w == 1 ? acc[1][t] : (w == 2 ? acc[2][t] : acc[3][t]));
#pragma unroll
            for (int sl = 0; sl < 3; ++sl) sm += *(const f32x4*)(lds + T_RED + (w * 3 + sl) * 4096 + t * 1024 + l16);
            pk[2 * t] = cvtpk(sm[0], sm[1]); pk[2 * t + 1] = cvtpk(sm[2], sm[3]);
        }
        unsigned short* orow = out + (size_t)(mb * 64 + w * 16 + (lane & 15)) * 1024 + nb * 64 + (lane >> 4) * 16;
        *(u32x4*)(orow) = u32x4{pk[0], pk[1], pk[2], pk[3]};
        *(u32x4*)(orow + 8) = u32x4{pk[4], pk[5], pk[6], pk[7]};
        return;
    }
    const int t8 = tid - 256, ch = rg >> 1, wi = rg & 1, t7 = t8 & 127;
    LAS unsigned* cnt = cnts + 1;
    CTX_SIGNAL()
    {
        const unsigned short* pl = (const unsigned short*)(lds + T_PRM);
        const float dt = bfround(expf(bf2f(pl[1536 + ch])));
        const int i16 = lane & 15, g4 = lane >> 4;
        char* scr = lds + T_UC + rg * 1024;
        {
#define PZ(i) bf2f(pl[(i) * 256 + ch * 128 + wi * 64 + lane])
            const cf coef = cmul(cf{PZ(4), PZ(5)}, cf{dt * PZ(2), dt * PZ(3)});
            const float mx = fmaxf(fabsf(coef.r), fabsf(coef.i)), sr = mx > 0.f ? coef.r / mx : 0.f, si = mx > 0.f ? coef.i / mx : 0.f;
            const float lc = log2f(mx) + 0.5f * log2f(sr * sr + si * si);
            *(f32x4*)(scr + l16) = f32x4{dt * PZ(0) * 1.4426950408889634f, dt * PZ(1) * 0.15915494309189535f, lc,
                                         atan2f(coef.i, coef.r) * 0.15915494309189535f};
        }
        asm volatile("" ::: "memory");
        const float vv[4] = {8.f * i16, (float)((i16 >> 3) * 128 + (i16 & 7)),
                             (float)(i16 < 8 ? 256 + (i16 >> 2) * 128 + (i16 & 3) * 2 : 512 + ((i16 >> 2) & 1) * 4 + (i16 & 3) * 128),
                             (float)(1024 + 128 * (i16 < 8 ? i16 : 8))};
        f32x4 res[3][2];
#pragma unroll
        for (int T = 0; T < 3; ++T) { res[T][0] = f32x4{}; res[T][1] = f32x4{}; }
#pragma unroll 2
        for (int q = 0; q < 16; ++q) {
            const f32x4 m = *(const f32x4*)(scr + (q * 4 + g4) * 16);
            const float fe = __builtin_amdgcn_exp2f(m[0] * vv[0]), fp = __builtin_amdgcn_fractf(m[1] * vv[0]);
            const float fr = fe * __builtin_amdgcn_cosf(fp), fi = -fe * __builtin_amdgcn_sinf(fp);
#pragma unroll
            for (int T = 1; T < 4; ++T) {
                const float ge = __builtin_amdgcn_exp2f(__builtin_fmaf(m[0], vv[T], m[2]));
                const float gp = __builtin_amdgcn_fractf(__builtin_amdgcn_fractf(m[1] * vv[T]) + m[3]);
                res[T - 1][0] = __builtin_amdgcn_mfma_f32_16x16x4f32(ge * __builtin_amdgcn_cosf(gp), fr, res[T - 1][0], 0, 0, 0);
                res[T - 1][1] = __builtin_amdgcn_mfma_f32_16x16x4f32(ge * __builtin_amdgcn_sinf(gp), fi, res[T - 1][1], 0, 0, 0);
            }
        }
        float* kd = (float*)(lds + T_KD + rg * KDB);
#pragma unroll
        for (int e = 0; e < 4; ++e) {
            const int i = g4 * 4 + e, j = i16;
            const int id1 = i < 8 ? 256 + (i >> 2) * 64 + (i & 3) + 4 * j : 384 + (i & 3) * 32 + ((i >> 2) & 1) + 2 * j;
            const int id2 = 512 + i * 16 + j;
            kd[(i >> 3) * 128 + (i & 7) + 8 * j] = res[0][0][e] + res[0][1][e];
            kd[id1] = res[1][0][e] + res[1][1][e];
            if (id2 <= 640) kd[id2] = res[2][0][e] + res[2][1][e];
        }
    }
    role_sync(cnt, 4, lane);
    char* kp = lds + T_KP + ch * 12288;
    char* uc = lds + T_UC + ch * (8 * UCOPY);
    {
        const float* kd0 = (const float*)(lds + T_KD + (ch * 2) * KDB), * kd1 = (const float*)(lds + T_KD + (ch * 2 + 1) * KDB);
        float kv[8];
        if (wi == 0) {
            const int base = lane < 32 ? 8 * lane : 128 + 4 * lane;
            const f32x4 a0 = *(const f32x4*)(kd0 + base) + *(const f32x4*)(kd1 + base);
            const f32x4 a1 = *(const f32x4*)(kd0 + base + 4) + *(const f32x4*)(kd1 + base + 4);
            const bool ex = lane < 32;
            kv[0] = a0[0]; kv[1] = ex ? a0[1] : a0[0]; kv[2] = ex ? a0[2] : a0[1]; kv[3] = ex ? a0[3] : a0[2];
            kv[4] = ex ? a1[0] : a0[2]; kv[5] = ex ? a1[1] : a0[2]; kv[6] = ex ? a1[2] : a0[3]; kv[7] = ex ? a1[3] : a1[0];
        } else {
            const int base = 256 + 2 * t7;
            const f32x2 a0 = *(const f32x2*)(kd0 + base) + *(const f32x2*)(kd1 + base);
            const float a2 = kd0[base + 2] + kd1[base + 2];
            kv[0] = kv[1] = kv[2] = a0[0]; kv[3] = kv[4] = kv[5] = a0[1]; kv[6] = kv[7] = a2;
        }
        hilo_store(kp, t7, kv);
        {
            const int c8 = 128 + t7, base = 384 + c8;
            const float a0 = kd0[base] + kd1[base], a1 = kd0[base + 1] + kd1[base + 1];
            kv[0] = kv[1] = kv[2] = kv[3] = a0; kv[4] = (c8 & 1) ? a1 : a0; kv[5] = kv[6] = kv[7] = a1;
            hilo_store(kp, c8, kv);
        }
        if (t7 < 64) *(u32x4*)(kp + (t7 >> 5) * 4608 + (t7 & 31) * 16) = u32x4{0, 0, 0, 0};
        const unsigned sel = ch ? 0x03020706u : 0x01000504u;
#pragma unroll 1
        for (int it = 0; it < 2; ++it) {
            const int X = t7 + it * 128;
            const char* ul = lds + T_U2 + X * 32 - 32;
            const u32x4 x0 = *(const u32x4*)(ul), x1 = *(const u32x4*)(ul + 16), x2 = *(const u32x4*)(ul + 32), x3 = *(const u32x4*)(ul + 48);
            const unsigned xx[16] = {x0[0], x0[1], x0[2], x0[3], x1[0], x1[1], x1[2], x1[3], x2[0], x2[1], x2[2], x2[3], x3[0], x3[1], x3[2], x3[3]};
            unsigned dd[8];
#pragma unroll
            for (int k = 0; k < 8; ++k) dd[k] = __builtin_amdgcn_perm(xx[15 - 2 * k], xx[14 - 2 * k], sel);
#pragma unroll
            for (int cc = 0; cc < 8; ++cc) {
                const int e = 7 - cc;
                u32x4 v;
#pragma unroll
                for (int k = 0; k < 4; ++k)
                    v[k] = (e & 1) ? __builtin_amdgcn_alignbit(dd[(e + 1) / 2 + k], dd[(e - 1) / 2 + k], 16) : dd[e / 2 + k];
                *(u32x4*)(uc + cc * UCOPY + (X + 3) * 16) = v;
            }
        }
        if (t7 < 24) *(u32x4*)(uc + (t7 & 7) * UCOPY + (t7 >> 3) * 16) = u32x4{0, 0, 0, 0};
    }
    role_sync(cnt, 8, lane);
    f32x4 y[8];
#pragma unroll
    for (int j = 0; j < 8; ++j) y[j] = f32x4{};
    {
        const int r16 = lane & 15, g4 = lane >> 4;
        unsigned ub = (unsigned)(uintptr_t)uc + (r16 & 7) * UCOPY + ((r16 >> 3) - g4 + 5) * 16 + wi * 256;
        unsigned kb = (unsigned)(uintptr_t)kp + (30 + 2 * r16 + g4) * 16 - wi * 256;
#pragma unroll 1
        for (int r = 0; r < 4; ++r) {
            bf16x8 ua[8], kh[2], kl[2];
            kh[0] = lds_rd128(kb); kl[0] = lds_rd128(kb + 4608);
#pragma unroll
            for (int m = 0; m < 8; ++m) ua[m] = lds_rd128(ub + m * 512);
#pragma unroll
            for (int n = 0; n < 8; ++n) {
                if (n < 7) { kh[(n + 1) & 1] = lds_rd128(kb + (n + 1) * 512); kl[(n + 1) & 1] = lds_rd128(kb + 4608 + (n + 1) * 512); }
#pragma unroll
                for (int m = 0; m + n < 8; ++m) y[m + n] = MFMA16(ua[m], kh[n & 1], y[m + n]);
#pragma unroll
                for (int m = 0; m + n < 8; ++m) y[m + n] = MFMA16(ua[m], kl[n & 1], y[m + n]);
                SBAR();
            }
            ub += 64; kb -= 64;
        }
    }
    role_sync(cnt, 12, lane);
    {
        char* xb = lds + T_UC + rg * 4096;
#pragma unroll
        for (int j = 0; j < 4; ++j) { const f32x4 ya = y[j], yb = y[4 + j]; *(f32x4*)(xb + j * 1024 + l16) = wi ? ya : yb; }
    }
    role_sync(cnt, 16, lane);
    {
        const char* xb = lds + T_UC + (rg ^ 1) * 4096;
        float* yo = (float*)(lds + T_UC + 16384) + ch * 2048 + wi * 1024 + (lane & 15) * 16 + (lane >> 4) * 4;
#pragma unroll
        for (int j = 0; j < 4; ++j) {
            const f32x4 ya = y[j], yb = y[4 + j];
            *(f32x4*)(yo + j * 256) = (wi ? yb : ya) + *(const f32x4*)(xb + j * 1024 + l16);
        }
    }
    role_sync(cnt, 20, lane);
    {
        const unsigned dsk = *(const unsigned*)(lds + T_PRM + 3076);
        const float ds0 = __uint_as_float(dsk << 16), ds1 = __uint_as_float(dsk & 0xffff0000u);
        const float* ya = (const float*)(lds + T_UC + 16384);
#pragma unroll 2
        for (int k = 0; k < 8; ++k) {
            const int l = t8 + 256 * k, yi = l;
            const unsigned upk = *(const unsigned*)(lds + T_U2 + l * 4);
            const float u0 = __uint_as_float(upk << 16), u1 = __uint_as_float(upk & 0xffff0000u);
            const float o0 = bfround(ya[yi]) + bfround(u0 * ds0);
            const float o1 = bfround(ya[2048 + yi]) + bfround(u1 * ds1);
            *(unsigned*)(out + (size_t)l * 1024 + 512 + d0) = cvtpk(o0, o1);
        }
    }
}

__global__ __launch_bounds__(512, 2) void mega_kernel(cbf x, cbf ckv, cbf wq, cbf wuk, cbf wuv, cbf wo, cbf u, cbf are, cbf aim, cbf bre,
                                                      cbf bim, cbf cre, cbf cim, cbf logdt, cbf dskip, char* __restrict__ ws,
                                                      unsigned short* __restrict__ out) {
    __shared__ __attribute__((aligned(16))) char lds[163840];
    u64 pc;
    asm volatile("s_getpc_b64 %0" : "=s"(pc));
    const int tid = threadIdx.x, lane = tid & 63, w = rfl(tid >> 6), r32 = lane & 31, hh = lane >> 5;
    const int b = blockIdx.x, xcd = b & 7, idx = b >> 3, h = xcd * 4 + (idx & 3), rb = idx >> 2;
    const int bi = rb, bj = 15 - rb, grp = w >> 2, rg = w & 3;
    const unsigned l16 = lane * 16;
    gu64* epoch = (gu64*)(ws + WS_FLAG);
    gu64* acks = (gu64*)(ws + WS_FLAG + 4096);
    gu32* ctr = (gu32*)(ws + WS_FLAG + 8192);
    AT c;
    c.ws = ws; c.h = h; c.bi = bi; c.bj = bj; c.grp = grp; c.rg = rg; c.qrel = (lane & 15) - 4 * (lane >> 4); c.w = w;
    c.lb = (unsigned)(uintptr_t)lds; c.l16 = l16;
    c.ldp = ws + WS_KV + ((size_t)h << 20) + w * 4096;
    c.qh = ws + WS_Q + ((size_t)b << 15);
    c.ldbase = c.lb + (grp ? 0 : 98304) + rg * 4096;
    c.ldoff = 0; c.ldlim = grp ? 98304 : 65536; c.koff = 16384; c.voff = 0; c.taun = 1; c.ldleft = 32 - 2 * bi;
    {
    constexpr unsigned S0 = 0u, S1 = 32768u, S2 = 65536u, S3 = 98304u, S4 = 131072u;
    PJ p;
    p.lb = c.lb;
    {
        const int r8 = lane >> 3, sl8 = lane & 7, r4 = lane >> 4, sl16 = lane & 15;
        p.xoff0 = r8 * 1024 + ((sl8 ^ (r8 >> 1)) << 4);
        p.xoff1 = r8 * 1024 + ((sl8 ^ (4 + (r8 >> 1))) << 4);
        p.woff[0] = r4 * 8192 + ((sl16 ^ ((r4 << 2) | ((2 * w) & 3))) << 4);
        p.woff[1] = r4 * 8192 + ((sl16 ^ ((r4 << 2) | ((2 * w + 1) & 3))) << 4);
    }
    p.xdst = p.lb + w * 4096; p.wdst = p.lb + w * 2048;
    const size_t wrow_off = (size_t)(w * 8) * 8192 + h * 256;
    p.xs = (const char*)ckv + (size_t)((rg * 8 + rb) * 64 + grp * 32) * 1024; p.w1 = (const char*)wuk + wrow_off; p.w2 = (const char*)wuv + wrow_off;
#define XADV() p.xs += 128
#define WADV() { p.w1 += 64 * 8192; p.w2 += 64 * 8192; }
    const int sh = idx * 8 + w;
    const size_t warm_x = (size_t)(sh * 8 + (lane & 7)) * 1024 + ((lane >> 3) & 1) * 128;
    const size_t warm_w = (size_t)(sh >> 1) * 8192 + xcd * 1024 + ((sh & 1) * 4 + (lane & 3)) * 128;
    p.winc = lane < 16 ? 256u : 128u * 8192u;
    p.wp = (lane < 16 ? (const char*)ckv + warm_x : (lane < 20 ? (const char*)wuk : (const char*)wuv) + warm_w) + (size_t)p.winc;
    p.dummy = 0;
    {
        const unsigned off = min((unsigned)(sh * 2 + (lane & 1)) * 128u, CODEWARM);
        asm volatile("s_mov_b64 exec, 3\n\tglobal_load_dword %0, %1, %2\n\ts_mov_b64 exec, -1"
                     : "+v"(p.dummy) : "v"(off), "s"(pc & ~127ull) : "memory");
    }
    const unsigned vzero = 0;
    u64 ep;
    asm volatile("global_load_dwordx2 %0, %1, %2 sc1" : "=v"(ep) : "v"(vzero), "s"(epoch) : "memory");
    if (b == 0 && w == 0) {
#pragma unroll
        for (int k = 0; k < 3; ++k) if (k * 64 + lane < C_NUM) __hip_atomic_store(ctr + (k * 64 + lane) * 32, 0u, RLX_AGENT);
    }
#pragma unroll
    for (int t = 0; t < 2; ++t) {
#pragma unroll
        for (int q = 0; q < 4; ++q) { pj_x(p, q, t ? S1 : S0); pj_w(p, q, t ? S3 : S2); }
        XADV(); WADV();
        if (t == 0) asm volatile("s_barrier" ::: "memory");
    }
    pj_warm(p);
    asm volatile("s_waitcnt vmcnt(9)" ::: "memory");
    PIN(ep);
    ep = ((u64)rfl((unsigned)(ep >> 32)) << 32) | rfl((unsigned)ep);
    if (tid == 0) __hip_atomic_store(acks + b, ack_of(ep, b), RLX_AGENT);
    const int r16 = lane & 15, g4 = lane >> 4, q4 = r16 >> 2, p4 = lane & 3;
#define WBASE(cc, t) (p.lb + (8 * g4 + 4 * (t) + q4) * 256 + (((2 * (cc) + (p4 >> 1)) ^ ((q4 << 2) | ((g4 & 1) << 1) | (t))) << 4) + (p4 & 1) * 8)
    const int th = w >> 2, dq = w & 3;
    const int tq = w >> 1, dh = w & 1;
#pragma unroll
    for (int s = 0; s < 2; ++s) p.xa[s] = p.lb + th * 16384 + r16 * 128 + (((4 * s + g4) ^ ((r16 >> 1) & 7)) << 4);
#pragma unroll
    for (int i = 0; i < 4; ++i) p.wa[i >> 1][i & 1] = WBASE(2 * dq + (i >> 1), i & 1);
    SYNC_LDS();
    FW f0, f1;
    bf16x8 xr[8];
    bf16x8 kvp[16];
    {
        f32x4 acck[8][2], accv[8][2];
#pragma unroll
        for (int i = 0; i < 16; ++i) { acck[i >> 1][i & 1] = f32x4{}; accv[i >> 1][i & 1] = f32x4{}; }
#pragma unroll
        for (int j = 0; j < 8; ++j) kvw_read(p, f0, j, 0, S2);
#pragma unroll
        for (int j = 0; j < 4; ++j) xr[j] = x_read(p, j, 0, S0);
#define KV_QUARTER(Q, fc, fn, xb, wb, xn, wn, RD, H1, H2, H3)                                                              \
        __builtin_amdgcn_s_setprio((Q) == 3 ? 3 : 2 - (Q));                                                               \
        _Pragma("unroll") for (int gi = 0; gi < 4; ++gi) {                                                                \
            const int G = 4 * (Q) + gi, tt = G & 7, N = G + 4;                                                            \
            if (N < 16) xr[N & 7] = x_read(p, N & 7, N >> 3, xb); else if (RD) xr[N & 7] = x_read(p, N & 7, 0, xn);       \
            acck[tt][0] = MFMA16(fw(fc, 0), xr[tt], acck[tt][0]); PIN(acck[tt][0]);                                       \
            SBAR();                                                                                                       \
            if ((Q) < 2) kvw_read(p, fn, G, 1, wb); else if ((Q) == 3 && RD) kvw_read(p, fn, 2 * gi, 0, wn);              \
            acck[tt][1] = MFMA16(fw(fc, 1), xr[tt], acck[tt][1]); PIN(acck[tt][1]);                                       \
            SBAR();                                                                                                       \
            if ((Q) == 3 && RD) kvw_read(p, fn, 2 * gi + 1, 0, wn);                                                       \
            accv[tt][0] = MFMA16(xr[tt], fw(fc, 2), accv[tt][0]); PIN(accv[tt][0]);                                       \
            SBAR();                                                                                                       \
            accv[tt][1] = MFMA16(xr[tt], fw(fc, 3), accv[tt][1]); PIN(accv[tt][1]);                                       \
            if (gi == 0) { H1; } if (gi == 2) { H2; } if (gi == 3) { H3; }                                                \
            SBAR();                                                                                                       \
        }
        KV_QUARTER(0, f0, f1, S0, S2, 0u, 0u, true, pj_warm(p), , )
        KV_QUARTER(1, f0, f1, S0, S2, 0u, 0u, true, pj_w(p, 0, S4), pj_w(p, 1, S4), )
        KV_QUARTER(2, f1, f0, S0, S2, 0u, 0u, true, pj_w(p, 2, S4), pj_w(p, 3, S4), WADV())
        SYNC_N(5);
        KV_QUARTER(3, f1, f0, S0, S2, S1, S3, true, pj_x(p, 0, S0), pj_x(p, 1, S0), )
        unsigned wcur = S3, wnext = S4, wld = S2;
#pragma unroll 1
        for (int t = 1; t < 6; ++t) {
            const unsigned xb = (t & 1) << 15, xn = xb ^ S1;
            if (t == 2) p.wp = lane < 16 ? (const char*)x + warm_x : (const char*)wq + warm_w;
            if (t == 2 && b == 0 && w == 0) {
                unsigned spins = 0;
                for (;;) {
                    bool ok = true;
#pragma unroll
                    for (int k = 0; k < 4; ++k) ok &= __hip_atomic_load(acks + k * 64 + lane, RLX_AGENT) == ack_of(ep, k * 64 + lane);
                    if (__all(ok) || ++spins >= SPIN_CAP) break;
                    __builtin_amdgcn_s_sleep(2);
                }
                asm volatile("s_waitcnt vmcnt(0)" ::: "memory");
                if (lane == 0) __hip_atomic_store(epoch, ep + 1, RLX_AGENT);
            }
            KV_QUARTER(0, f0, f1, xb, wcur, 0u, 0u, true, pj_x(p, 2, xn), pj_x(p, 3, xn); XADV(), pj_warm(p))
            KV_QUARTER(1, f0, f1, xb, wcur, 0u, 0u, true, pj_w(p, 0, wld), pj_w(p, 1, wld), )
            KV_QUARTER(2, f1, f0, xb, wcur, 0u, 0u, true, pj_w(p, 2, wld), pj_w(p, 3, wld), WADV())
            SYNC_N(5);
            KV_QUARTER(3, f1, f0, xb, wcur, xn, wnext, true, pj_x(p, 0, xb), pj_x(p, 1, xb), )
            const unsigned tmp = wcur; wcur = wnext; wnext = wld; wld = tmp;
        }
        SBAR();
#define WO_DMA(q) glds1((const char*)wo + (size_t)((b >> 1) * 32 + (w * 2 + (q)) * 2) * 1024 + (b & 1) * 512, (lane >> 5) * 1024 + (lane & 31) * 16, p.lb + S4 + 16384 + (w * 2 + (q)) * 1024)
        p.w1 = (const char*)wq + wrow_off;
        KV_QUARTER(0, f0, f1, S0, S2, 0u, 0u, true, pj_x(p, 2, S1), pj_x(p, 3, S1), )
        p.xs = (const char*)x + (size_t)((grp ? bj : bi) * 128 + rg * 32) * 1024;
        KV_QUARTER(1, f0, f1, S0, S2, 0u, 0u, true, pj_w(p, 0, S4), pj_w(p, 1, S4), WADV())
        KV_QUARTER(2, f1, f0, S0, S2, 0u, 0u, true, WO_DMA(0), WO_DMA(1), )
        SYNC_N(4);
        KV_QUARTER(3, f1, f0, S0, S2, S1, S3, true, pj_x(p, 0, S0), pj_x(p, 1, S0), )
        KV_QUARTER(0, f0, f1, S1, S3, 0u, 0u, true, pj_x(p, 2, S0), pj_x(p, 3, S0), XADV())
        KV_QUARTER(1, f0, f1, S1, S3, 0u, 0u, true, pj_w(p, 0, S2), pj_w(p, 1, S2), WADV())
        KV_QUARTER(2, f1, f0, S1, S3, 0u, 0u, true, pj_w(p, 0, S2 + 16384), pj_w(p, 1, S2 + 16384), WADV())
        SYNC_N(4);
        asm volatile("" :: "v"(p.dummy));
        KV_QUARTER(3, f1, f0, S1, S3, 0u, 0u, false, pj_x(p, 0, S1), pj_x(p, 1, S1), )
#pragma unroll
        for (int i = 0; i < 8; ++i) p.wa[i >> 1][i & 1] = WBASE(4 * dh + (i >> 1), i & 1);
#pragma unroll
        for (int s = 0; s < 2; ++s) p.xa[s] += tq * 8192 - th * 16384;
#pragma unroll
        for (int j = 0; j < 8; ++j) qw_read(p, f0, j, 0, S4);
#pragma unroll
        for (int j = 0; j < 2; ++j) xr[j] = x_read(p, j, 0, S0);
#pragma unroll
        for (int tb = 0; tb < 4; ++tb)
#pragma unroll
            for (int i = 0; i < 2; ++i) {
                kvp[tb * 4 + i * 2] = pack2(acck[2 * tb + i][0], acck[2 * tb + i][1]);
                kvp[tb * 4 + i * 2 + 1] = pack2(accv[2 * tb][i], accv[2 * tb + 1][i]);
                PIN(kvp[tb * 4 + i * 2]); PIN(kvp[tb * 4 + i * 2 + 1]);
            }
    }
    {
        f32x4 acc[4][4];
#pragma unroll
        for (int i = 0; i < 16; ++i) acc[i >> 2][i & 3] = f32x4{};
        char* kvt = ws + WS_KV + ((size_t)(h * 32 + rb) << 15);
#define KVST(i) st16(kvt + ((size_t)((i) >> 2) << 18) + (((i) & 1) ? 16384 + (((dq * 2 + (((i) >> 1) & 1)) * 2 + th) << 10)            \
                                                                  : (((th * 2 + (((i) >> 1) & 1)) * 4 + dq) << 10)), l16, kvp[i])
#define KVSIG(g) if (w == 0) atomic_inc1(ws, C_KV + h * 4 + (g), l16, 1u);
#define WO_ST(it) { const int f = w + (it) * 8; const unsigned a = p.lb + S4 + 16384 + (4 * g4 + q4) * 512 + (f >> 2) * 128 + p4 * 32 + (f & 3) * 8;   \
                    st16(ws + WS_WO + ((size_t)(((b & 1) * 16 + f) * 128 + (b >> 1)) << 10), l16, cat8(lds_tr(a), lds_tr(a + 8192))); }
#define U_DMA() glds1((const char*)u + (size_t)((b >> 2) * 32 + w * 4) * 1024 + (b & 3) * 256, (lane >> 4) * 1024 + (lane & 15) * 16, p.lb + S4 + 16384 + w * 1024)
#define U_ST() { const char* us = lds + S4 + 16384 + (tid & 7) * 1024 + (tid >> 3) * 4;                                                  \
                 const u32x4 v = {*(const unsigned*)us, *(const unsigned*)(us + 256), *(const unsigned*)(us + 512), *(const unsigned*)(us + 768)};   \
                 st16(ws + WS_UT + (size_t)((b & 3) * 64) * 8192 + (b >> 2) * 128, (tid >> 3) * 8192 + (tid & 7) * 16, __builtin_bit_cast(bf16x8, v)); }
#define Q_STEP(Q, fc, fn, xb, wb, xn, wn, RD, H0, H1, H2, H3)                                                             \
        __builtin_amdgcn_s_setprio((Q) == 3 ? 3 : 2 - (Q));                                                               \
        _Pragma("unroll") for (int gi = 0; gi < 2; ++gi) {                                                                \
            const int G = 2 * (Q) + gi, tt = G & 3, N = G + 2;                                                            \
            if (N < 8) xr[N & 3] = x_read(p, N & 3, N >> 2, xb); else if (RD) xr[N & 3] = x_read(p, N & 3, 0, xn);        \
            _Pragma("unroll") for (int j = 0; j < 4; ++j) {                                                               \
                if ((Q) < 2) { if (j & 1) qw_read(p, fn, 2 * G + (j >> 1), 1, opaque(wb)); }                              \
                else if ((Q) == 3 && RD) qw_read(p, fn, 4 * gi + j, 0, opaque(wn));                                       \
                acc[tt][j] = MFMA16(fw(fc, j), xr[tt], acc[tt][j]); PIN(acc[tt][j]);                                      \
                if (j == 1) { if (gi == 0) { H0; } else { H2; } } if (j == 3) { if (gi == 0) { H1; } else { H3; } }       \
                SBAR();                                                                                                   \
            }                                                                                                             \
        }
        constexpr unsigned W0 = S4, W1 = S2, W2 = S2 + 16384;
        u64 ep2; unsigned kvc;
        asm volatile("global_load_dwordx2 %0, %1, %2 sc1" : "=v"(ep2) : "v"(vzero), "s"(epoch) : "memory");
        Q_STEP(0, f0, f1, S0, W0, 0u, 0u, true, KVST(0); KVST(1), KVST(2); KVST(3), pj_x(p, 2, S1), pj_x(p, 3, S1); XADV())
        Q_STEP(1, f0, f1, S0, W0, 0u, 0u, true, pj_x(p, 0, S3), , pj_x(p, 1, S3), )
        Q_STEP(2, f1, f0, S0, W0, 0u, 0u, true, pj_x(p, 2, S3), , pj_x(p, 3, S3); XADV(), )
        SYNC_N(4);
        PIN(ep2);
        if (w == 0) {
            unsigned spins = 0;
            while (ep2 != ep + 1 && ++spins < SPIN_CAP) { __builtin_amdgcn_s_sleep(2); ep2 = __hip_atomic_load(epoch, RLX_AGENT); }
        }
        KVSIG(0)
        Q_STEP(3, f1, f0, S0, W0, S1, W1, true, pj_x(p, 0, S0), , pj_x(p, 1, S0), )
        Q_STEP(0, f0, f1, S1, W1, 0u, 0u, true, pj_x(p, 2, S0), , pj_x(p, 3, S0); XADV(), )
        Q_STEP(1, f0, f1, S1, W1, 0u, 0u, true, pj_w(p, 0, W0), , KVST(4), WO_ST(0))
        Q_STEP(2, f1, f0, S1, W1, 0u, 0u, true, pj_w(p, 1, W0); WADV(), , , WO_ST(1))
        SYNC_N(9);
        Q_STEP(3, f1, f0, S1, W1, S3, W2, true, pj_x(p, 0, S1), , pj_x(p, 1, S1), )
        Q_STEP(0, f0, f1, S3, W2, 0u, 0u, true, pj_x(p, 2, S1), , pj_x(p, 3, S1); XADV(), )
        Q_STEP(1, f0, f1, S3, W2, 0u, 0u, true, pj_w(p, 0, W1), , KVST(5), )
        Q_STEP(2, f1, f0, S3, W2, 0u, 0u, true, pj_w(p, 1, W1); WADV(), , , )
        SYNC_N(8);
        Q_STEP(3, f1, f0, S3, W2, S0, W0, true, pj_x(p, 0, S3), , pj_x(p, 1, S3), U_DMA())
        Q_STEP(0, f0, f1, S0, W0, 0u, 0u, true, pj_x(p, 2, S3), , pj_x(p, 3, S3); XADV(), )
        Q_STEP(1, f0, f1, S0, W0, 0u, 0u, true, pj_w(p, 0, W2), , KVST(6), )
        Q_STEP(2, f1, f0, S0, W0, 0u, 0u, true, pj_w(p, 1, W2); WADV(), , , )
        SYNC_N(8);
        Q_STEP(3, f1, f0, S0, W0, S1, W1, true, pj_x(p, 0, S0), , pj_x(p, 1, S0), )
        asm volatile("global_load_dword %0, %1, %2 sc1" : "=v"(kvc) : "v"((unsigned)(C_KV + h * 4) * 128u), "s"(ws + WS_FLAG + 8192) : "memory");
        Q_STEP(0, f0, f1, S1, W1, 0u, 0u, true, pj_x(p, 2, S0), , pj_x(p, 3, S0); XADV(), )
        Q_STEP(1, f0, f1, S1, W1, 0u, 0u, true, pj_w(p, 0, W0), , KVST(7), )
        Q_STEP(2, f1, f0, S1, W1, 0u, 0u, true, pj_w(p, 1, W0); WADV(), , , )
        SYNC_N(5);
        PIN(kvc);
        if (w == 0) {
            if (rfl(kvc) < 8u) wait_ge(ctr + (C_KV + h * 4) * 32, 8u);
        }
        Q_STEP(3, f1, f0, S1, W1, S3, W2, true, pj_x(p, 0, S1), , pj_x(p, 1, S1), )
        Q_STEP(0, f0, f1, S3, W2, 0u, 0u, true, pj_x(p, 2, S1), , pj_x(p, 3, S1), )
        Q_STEP(1, f0, f1, S3, W2, 0u, 0u, true, pj_w(p, 0, W1), , , )
        Q_STEP(2, f1, f0, S3, W2, 0u, 0u, true, pj_w(p, 1, W1), , , )
        SYNC_N(6);
        KVSIG(1)
#define KSITE(q) if (!grp) { at_piece(c, (q) & 1); if ((q) & 1) at_adv(c); }
#define ASITE(q) { at_piece(c, (q) & 1); if ((q) & 1) at_adv(c); }
        Q_STEP(3, f1, f0, S3, W2, S0, W0, true, KSITE(0), , , )
        const int d0 = xcd * 64 + idx * 2;
        const char* s4p[2];
#pragma unroll
        for (int k = 0; k < 2; ++k) {
            const int e = tid + 512 * k, ar = e >> 7;
            const char* bp = ar == 0 ? (const char*)are : ar == 1 ? (const char*)aim : ar == 2 ? (const char*)bre : ar == 3 ? (const char*)bim :
                             ar == 4 ? (const char*)cre : (const char*)cim;
            s4p[k] = e < 768 ? bp + d0 * 256 + (e & 127) * 4 : (e == 768 ? (const char*)logdt : (const char*)dskip) + d0 * 2;
        }
#define S4P(k) asm volatile("global_load_dword %0, %1, off" : "=v"(c.s4[k]) : "v"(s4p[k]) : "memory")
        Q_STEP(0, f0, f1, S0, W0, 0u, 0u, true, KSITE(1), , KVST(8), )
        Q_STEP(1, f0, f1, S0, W0, 0u, 0u, true, KSITE(2), , U_ST(), )
        Q_STEP(2, f1, f0, S0, W0, 0u, 0u, true, KSITE(3), , KVST(9), )
        if (grp) asm volatile("s_waitcnt vmcnt(3)" ::: "memory"); else asm volatile("s_waitcnt vmcnt(11)" ::: "memory");
        SYNC_LDS();
        Q_STEP(3, f1, f0, S0, W0, S1, W1, true, ASITE(0), , , )
        Q_STEP(0, f0, f1, S1, W1, 0u, 0u, true, ASITE(1), , KVST(10), )
        Q_STEP(1, f0, f1, S1, W1, 0u, 0u, true, ASITE(2), , KVST(11), )
        Q_STEP(2, f1, f0, S1, W1, 0u, 0u, true, ASITE(3), S4P(0), , S4P(1))
        SYNC_LDS();
        Q_STEP(3, f1, f0, S1, W1, 0u, 0u, false, , , , )
        asm volatile("" :: "v"(p.dummy));
#pragma unroll
        for (int tt = 0; tt < 4; ++tt) {
            const int tk = tq * 2 + (tt >> 1);
            char* xq = lds + S1 + tk * 8192 + l16;
#pragma unroll
            for (int i = 0; i < 2; ++i) {
                const int sfr = (tt & 1) * 4 + dh * 2 + i;
                const bf16x8 v = pack2(acc[tt][2 * i] * QSCALE, acc[tt][2 * i + 1] * QSCALE);
                *(bf16x8*)(xq + sfr * 1024) = v;
                if (tq >= 2) *(bf16x8*)(ws + WS_Q + ((size_t)b << 15) + (tk - 4) * 8192 + sfr * 1024 + l16) = v;
            }
        }
        if (grp) asm volatile("s_waitcnt vmcnt(16)" ::: "memory"); else asm volatile("s_waitcnt vmcnt(13)" ::: "memory");
        SYNC_LDS();
        if (w == 0) atomic_inc1(ws, C_WO, l16, 1u);
        KVST(12); KVST(13); KVST(14); KVST(15);
    }
    }
    const int blk1 = grp ? bj : bi;
    c.kvoff = (C_KV + h * 4 + 1) * 128; c.kvneed = 32 - 2 * bi - 8 + 3; c.kvc = 0;
    c.sigid = C_KV + h * 4 + 2; c.sign = 3;
#pragma unroll
    for (int s = 0; s < 8; ++s) c.qf[s >> 2][s & 3] = lds_rd128(c.lb + 32768 + w * 8192 + s * 1024 + l16);
#pragma unroll
    for (int qt = 0; qt < 2; ++qt) {
        c.negm[qt] = f32x4{}; c.mref[qt] = 0.f; c.ls[qt] = f32x4{}; PIN(c.mref[qt]);
#pragma unroll
        for (int i = 0; i < 8; ++i) c.o[i][qt] = f32x4{};
    }
    c.ones = __builtin_bit_cast(bf16x8, u32x4{0x3f803f80u, 0x3f803f80u, 0x3f803f80u, 0x3f803f80u}); PIN(c.ones);
    SB sA, sB;
    {
        const unsigned ks_ = c.lb + 98304 + l16;
#pragma unroll
        for (int i = 0; i < 16; ++i) {
            const bf16x8 kf = lds_rd128(ks_ + kitem(i));
            const int kt = i & 3, ds = i >> 2;
            sA.s[kt][0] = MFMA16(kf, c.qf[0][ds], ds ? sA.s[kt][0] : f32x4{});
            sA.s[kt][1] = MFMA16(kf, c.qf[1][ds], ds ? sA.s[kt][1] : f32x4{});
        }
#pragma unroll
        for (int i = 0; i < NPRE; ++i) c.fr[i] = lds_rd128(ks_ + 16384 + kitem(i));
        at_mask(c, sA, 0, blk1);
        at_check(c, sA, 0.f, true);
    }
#define PIN_STATE() { PIN(c.mref[0]); PIN(c.mref[1]); }
#pragma unroll 1
    for (int t = 0; t < bi; ++t) {
        at_step<0>(c, sA, sB, 1, 1, blk1, 0);
        at_step<0>(c, sB, sA, 1, 1, blk1, 0);
    }
    PIN_STATE()
    at_step<1>(c, sA, sB, 1, 1 + grp, blk1, 1);
    at_step<2>(c, sB, sA, 1 + grp, 2, bj, 1);
    PIN_STATE()
#pragma unroll 1
    for (int t = 0; t < 7 - bi; ++t) {
        at_step<0>(c, sA, sB, 2, 2, bj, 1);
        at_step<0>(c, sB, sA, 2, 2, bj, 1);
    }
    PIN_STATE()
    unsigned ticket = 0;
    if (w == 0 && c.l16 == 0) ticket = __hip_atomic_fetch_add(ctr + (C_TICK + xcd) * 32, 1u, RLX_AGENT);
    at_x<false>(c, sA, sB);
    at_bar<false>(c);
    tail_part(c, sB, lds, ticket);
}

extern "C" void kernel_launch(void* const* d_in, const int* in_sizes, int n_in, void* d_out, int out_size, void* d_ws,
                              size_t ws_size, hipStream_t stream) {
    (void)in_sizes; (void)n_in; (void)out_size; (void)ws_size;
    const unsigned short* const* in = (const unsigned short* const*)d_in;
    mega_kernel<<<256, 512, 0, stream>>>(in[0], in[1], in[2], in[3], in[4], in[5], in[6], in[7], in[8], in[9], in[10], in[11],
                                         in[12], in[13], in[14], (char*)d_ws, (unsigned short*)d_out);
}
```
